```python
import torch

def _softmax(x, axis=-1):
    return torch.softmax(x, dim=axis)

def _gelu(x):
    return torch.nn.functional.gelu(x, approximate='tanh')

def _silu(x):
    return torch.nn.functional.silu(x)

def _sigmoid(x):
    return torch.sigmoid(x)

def _layernorm(x, axis=-1, eps=1e-05):
    m = x.mean(dim=axis, keepdim=True)
    v = x.var(dim=axis, keepdim=True, unbiased=False)
    return (x - m) / torch.sqrt(v + eps)

def _scatter_add(tgt, idx, src, axis=0):
    return tgt.clone().index_add_(axis, idx, src)

def _segment_sum(src, seg, n):
    out = torch.zeros((n,) + src.shape[1:], dtype=src.dtype)
    return out.index_add_(0, seg, src)

def _pad(x, pw):
    flat = tuple((v for pair in reversed(pw) for v in pair))
    return torch.nn.functional.pad(x, flat)
kernel_name = 'concat_heads__ssm_conv_h3__memory_aug_recurrent_mem_tr__prompt'

def setup_inputs():

    _w = lambda a: (lambda t: t.to(torch.bfloat16) if t.is_floating_point() else t)(torch.as_tensor(a))

    def _A_setup():
        B, S, D = (1, 2048, 512)
        ks = 4
        return {'q': _w((lambda *s: torch.randn(*s, dtype=torch.bfloat16) * 0.05)(B, S, D)), 'k': _w((lambda *s: torch.randn(*s, dtype=torch.bfloat16) * 0.4)(B, S, D)), 'v': _w((lambda *s: torch.randn(*s, dtype=torch.bfloat16) * 0.4)(B, S, D)), 'shift_k': _w((lambda *s: torch.randn(*s, dtype=torch.bfloat16) * 0.4)(D, ks) * 0.5), 'ssm_k': _w((lambda *s: torch.randn(*s, dtype=torch.bfloat16) * 0.4)(D, S) * (1.0 / S ** 0.5)), 'ssm_D': _w((lambda *s: torch.randn(*s, dtype=torch.bfloat16) * 0.4)(D))}
    _A_in = _A_setup()

    def _B_setup():
        B, S, Dm = (1, 2048, 512)
        H, Dh = (32, 128)
        M, seg = (8, 32)
        seg = min(seg, S)
        S = S // seg * seg
        return {'x': _w((lambda *s: torch.randn(*s, dtype=torch.bfloat16) * 0.05)(B, S, Dm)), 'mem0': _w((lambda *s: torch.randn(*s, dtype=torch.bfloat16) * 0.4)(B, M, Dm)), 'Wq': _w((lambda *s: torch.randn(*s, dtype=torch.bfloat16) * 6.4)(Dm, H, Dh) * Dm ** (-0.5)), 'Wk': _w((lambda *s: torch.randn(*s, dtype=torch.bfloat16) * 6.4)(Dm, H, Dh) * Dm ** (-0.5)), 'Wv': _w((lambda *s: torch.randn(*s, dtype=torch.bfloat16) * 0.4)(Dm, H, Dh) * Dm ** (-0.5)), 'Wo': _w((lambda *s: torch.randn(*s, dtype=torch.bfloat16) * 0.4)(H, Dh, Dm) * (H * Dh) ** (-0.5)), 'seg_len': seg}
    _B_in = _B_setup()
    return {'A_q': _A_in['q'], 'A_k': _A_in['k'], 'A_v': _A_in['v'], 'A_shift_k': _A_in['shift_k'], 'A_ssm_k': _A_in['ssm_k'], 'A_ssm_D': _A_in['ssm_D'], 'B_x': _B_in['x'], 'B_mem0': _B_in['mem0'], 'B_Wq': _B_in['Wq'], 'B_Wk': _B_in['Wk'], 'B_Wv': _B_in['Wv'], 'B_Wo': _B_in['Wo'], 'B_seg_len': _B_in['seg_len']}

def reference(A_q, A_k, A_v, A_shift_k, A_ssm_k, A_ssm_D, B_x, B_mem0, B_Wq, B_Wk, B_Wv, B_Wo, B_seg_len):

    def _A_ref(q, k, v, shift_k, ssm_k, ssm_D):
        B, S, D = q.shape
        ks = shift_k.shape[-1]
        k_pad = _pad(k, ((0, 0), (ks - 1, 0), (0, 0)))
        k_shift = torch.zeros_like(k)
        for t in range(ks):
            k_shift = k_shift + k_pad[:, t:t + S, :] * shift_k[:, ks - 1 - t][None, None, :]
        kv = k_shift * v
        L = 2 * S
        kv_pad = _pad(kv, ((0, 0), (0, S), (0, 0)))
        kf_pad = _pad(ssm_k, ((0, 0), (0, S)))
        kv_f = (lambda x, axes=(-2, -1): torch.fft.rfft2(x.float(), dim=axes))(kv_pad[:, None, :, :], axes=(1, 2))
        kf_f = (lambda x, axes=(-2, -1): torch.fft.rfft2(x.float(), dim=axes))(kf_pad[:, None, :], axes=(1, 2))
        kf_f = (lambda x, *d: x.permute(*d))(kf_f, 1, 2, 0)
        y_f = kv_f * kf_f[None, :, :, :]
        y = (lambda x, s, axes=(-2, -1): torch.fft.irfft2(x, s=s, dim=axes).to(torch.bfloat16))(y_f, (1, L), axes=(1, 2))[:, 0, :S, :]
        y = y + kv * ssm_D[None, None, :]
        return q * y

    def _B_ref(x, mem0, Wq, Wk, Wv, Wo, seg_len):
        B, S, Dm = x.shape
        H, Dh = (Wq.shape[1], Wq.shape[2])
        M = mem0.shape[1]
        scale = Dh ** (-0.5)
        n_seg = S // seg_len
        L = M + seg_len
        idx = torch.arange(L)
        mask = (idx[:, None] < M) | (idx[:, None] >= idx[None, :])
        mem = mem0
        outs = []
        for s in range(n_seg):
            xs = x[:, s * seg_len:(s + 1) * seg_len, :].clone()
            h = torch.concatenate([mem, xs], axis=1)
            q = torch.einsum('btd,dhe->bhte', h, Wq)
            k = torch.einsum('btd,dhe->bhte', h, Wk)
            v = torch.einsum('btd,dhe->bhte', h, Wv)
            scores = torch.einsum('bhqe,bhke->bhqk', q, k) * scale
            scores = torch.where(mask[None, None], scores, -1000000000.0)
            p = _softmax(scores, axis=-1)
            o = torch.einsum('bhqk,bhke->bhqe', p, v)
            o = torch.einsum('bhte,hed->btd', o, Wo)
            mem = o[:, :M, :].clone()
            outs.append(o[:, M:, :])
        return torch.concatenate(outs, axis=1)
    a = _A_ref(A_q, A_k, A_v, A_shift_k, A_ssm_k, A_ssm_D)
    b = _B_ref(B_x, B_mem0, B_Wq, B_Wk, B_Wv, B_Wo, B_seg_len)
    return torch.concatenate([a, b], axis=-1)
```

```cpp
#include <hip/hip_runtime.h>
#include <stdint.h>

typedef unsigned v2u __attribute__((ext_vector_type(2)));
typedef unsigned v4u __attribute__((ext_vector_type(4)));
typedef unsigned v8u __attribute__((ext_vector_type(8)));
typedef float f32x4 __attribute__((ext_vector_type(4)));
typedef float f32x16 __attribute__((ext_vector_type(16)));
typedef __bf16 bf16x2 __attribute__((ext_vector_type(2)));
typedef __bf16 bf16x8 __attribute__((ext_vector_type(8)));
typedef short s16x4 __attribute__((ext_vector_type(4)));
typedef __attribute__((address_space(3))) s16x4* lds_s16x4_p;
typedef __attribute__((address_space(3))) char* lds_char_p;

#define DI __device__ __forceinline__

constexpr int NSEG = 64;
constexpr unsigned OFF_INB = 0;
constexpr unsigned OFF_MEM = 2u << 20;
constexpr unsigned OFF_FLG = OFF_MEM + 49152;
constexpr unsigned INIT_BYTES = (2u << 20) + 65536;
constexpr unsigned FLG_KV = OFF_FLG;
constexpr unsigned FLG_M = OFF_FLG + 8192;
constexpr unsigned FLG_O = OFF_FLG + 12288;
constexpr unsigned OFF_KX = 4u << 20;
constexpr unsigned OFF_VX = 20u << 20;
constexpr unsigned OFF_OX = 36u << 20;
constexpr unsigned OFF_KM = 52u << 20;
constexpr unsigned OFF_VM = 56u << 20;
constexpr unsigned WS_SPAN = 60u << 20;
constexpr unsigned POIS = 0xffffffffu;
constexpr int AUX_SC1 = 16;
constexpr unsigned SPIN_LIMIT = 1u << 21;

typedef __amdgpu_buffer_rsrc_t rsrc_t;
DI rsrc_t mkrsrc(const void* p, unsigned bytes) { return __builtin_amdgcn_make_buffer_rsrc((void*)p, 0, bytes, 0x00020000); }
DI v4u mksrd(const void* p, unsigned bytes) {
  uint64_t a = (uint64_t)p;
  v4u r = {(unsigned)a, (unsigned)(a >> 32) & 0xffffu, bytes, 0x00020000u};
  return r;
}
DI v4u ld16(rsrc_t r, unsigned off) { return __builtin_amdgcn_raw_buffer_load_b128(r, off, 0, AUX_SC1); }
DI v2u ld8(rsrc_t r, unsigned off) { return __builtin_amdgcn_raw_buffer_load_b64(r, off, 0, AUX_SC1); }
DI unsigned ld4(rsrc_t r, unsigned off) { return __builtin_amdgcn_raw_buffer_load_b32(r, off, 0, AUX_SC1); }
DI void st16(rsrc_t r, unsigned off, v4u v) { __builtin_amdgcn_raw_buffer_store_b128(v, r, off, 0, AUX_SC1); }
DI void st8(rsrc_t r, unsigned off, v2u v) { __builtin_amdgcn_raw_buffer_store_b64(v, r, off, 0, AUX_SC1); }
DI void st4(rsrc_t r, unsigned off, unsigned v) { __builtin_amdgcn_raw_buffer_store_b32(v, r, off, 0, AUX_SC1); }
DI v4u ldp16(rsrc_t r, unsigned off) { return __builtin_amdgcn_raw_buffer_load_b128(r, off, 0, 0); }
DI void stp16(rsrc_t r, unsigned off, v4u v) { __builtin_amdgcn_raw_buffer_store_b128(v, r, off, 0, 0); }

DI void dma16(v4u srd, unsigned voff, unsigned soff, unsigned ldsaddr) {
  asm volatile("s_mov_b32 m0, %3\n\ts_nop 0\n\tbuffer_load_dwordx4 %0, %1, %2 offen lds"
               :: "v"(voff), "s"(srd), "s"(soff), "s"(ldsaddr) : "memory");
}
DI void bar() { asm volatile("s_waitcnt lgkmcnt(0)\n\ts_barrier" ::: "memory"); }
template <int N> DI void wait_vm() { asm volatile("s_waitcnt vmcnt(%0)" :: "i"(N) : "memory"); }
DI v2u trd(const char* lds, unsigned off) {
  return __builtin_bit_cast(v2u, __builtin_amdgcn_ds_read_tr16_b64_v4i16((lds_s16x4_p)(lds + off)));
}
DI unsigned pk(float a, float b) { bf16x2 v = {(__bf16)a, (__bf16)b}; return __builtin_bit_cast(unsigned, v); }
DI float bflo(unsigned u) { return __uint_as_float(u << 16); }
DI float bfhi(unsigned u) { return __uint_as_float(u & 0xffff0000u); }
DI float rbf(float a) { return bflo(pk(a, a)); }
DI f32x4 mfma16(v4u a, v4u b, f32x4 c) {
  return __builtin_amdgcn_mfma_f32_16x16x32_bf16(__builtin_bit_cast(bf16x8, a), __builtin_bit_cast(bf16x8, b), c, 0, 0, 0);
}
DI float xmax16(float x) {
  auto r = __builtin_amdgcn_permlane16_swap(__float_as_uint(x), __float_as_uint(x), false, false);
  return fmaxf(__uint_as_float(r[0]), __uint_as_float(r[1]));
}
DI float xmax32(float x) {
  auto r = __builtin_amdgcn_permlane32_swap(__float_as_uint(x), __float_as_uint(x), false, false);
  return fmaxf(__uint_as_float(r[0]), __uint_as_float(r[1]));
}
DI float xsum16(float x) {
  auto r = __builtin_amdgcn_permlane16_swap(__float_as_uint(x), __float_as_uint(x), false, false);
  return __uint_as_float(r[0]) + __uint_as_float(r[1]);
}
DI float xsum32(float x) {
  auto r = __builtin_amdgcn_permlane32_swap(__float_as_uint(x), __float_as_uint(x), false, false);
  return __uint_as_float(r[0]) + __uint_as_float(r[1]);
}
DI float swapadd32(float a, float b) {
  auto r = __builtin_amdgcn_permlane32_swap(__float_as_uint(a), __float_as_uint(b), false, false);
  return __uint_as_float(r[0]) + __uint_as_float(r[1]);
}
DI unsigned umax4(v4u g) { return max(max(g.x, g.y), max(g.z, g.w)); }
DI int wait_flag(rsrc_t ws, unsigned off, int need, unsigned& spins) {
  int v;
  while (true) {
    asm volatile("" ::: "memory");
    v = (int)__builtin_amdgcn_readfirstlane(ld4(ws, off));
    if (v >= need) break;
    if (++spins > SPIN_LIMIT) break;
    __builtin_amdgcn_s_sleep(2);
  }
  return v;
}
constexpr float SCALE = 0.08838834764831845f;
constexpr float LOG2E = 1.4426950408889634f;
constexpr float NEG_MASK = -998244352.0f;

constexpr unsigned CH_MEM = 0, CH_QT = 8192, CH_KT = 10240, CH_OL = 12288, CH_KX = 14336, CH_WV = 32768;

#define SMF4(c0, c1, c2, c3, a, b0, b1, b2, b3, idx, PRE)                                     \
  asm(PRE "v_smfmac_f32_16x16x64_bf16 %0, %4, %5, %9\n\t"                                      \
      "v_smfmac_f32_16x16x64_bf16 %1, %4, %6, %9\n\t"                                          \
      "v_smfmac_f32_16x16x64_bf16 %2, %4, %7, %9\n\t"                                          \
      "v_smfmac_f32_16x16x64_bf16 %3, %4, %8, %9"                                              \
      : "+v"(c0), "+v"(c1), "+v"(c2), "+v"(c3)                                                 \
      : "v"(a), "a"(b0), "a"(b1), "a"(b2), "a"(b3), "v"(idx))
#define SMF2(c0, c1, a, b0, b1, idx, PRE)                                                      \
  asm(PRE "v_smfmac_f32_16x16x64_bf16 %0, %2, %3, %5\n\t"                                      \
      "v_smfmac_f32_16x16x64_bf16 %1, %2, %4, %5"                                              \
      : "+v"(c0), "+v"(c1) : "v"(a), "v"(b0), "v"(b1), "v"(idx))
#define SMFO(c0, c1, a0, a1, b00, b01, b10, b11, idx)                                          \
  asm("s_nop 1\n\t"                                                                            \
      "v_smfmac_f32_16x16x64_bf16 %0, %2, %4, %8\n\t"                                          \
      "v_smfmac_f32_16x16x64_bf16 %1, %2, %6, %8\n\t"                                          \
      "v_smfmac_f32_16x16x64_bf16 %0, %3, %5, %8\n\t"                                          \
      "v_smfmac_f32_16x16x64_bf16 %1, %3, %7, %8"                                              \
      : "+v"(c0), "+v"(c1) : "v"(a0), "v"(a1), "v"(b00), "v"(b01), "v"(b10), "v"(b11), "v"(idx))
#define MFENCE4(a, b, c, d) asm("s_nop 7" : "+v"(a), "+v"(b), "+v"(c), "+v"(d))
#define MFENCE3(a, b, c) asm("s_nop 7" : "+v"(a), "+v"(b), "+v"(c))
#define MFENCE2(a, b) asm("s_nop 7" : "+v"(a), "+v"(b))
#define MF3(c0, c1, c2, a0, a1, a2, b, PRE)                                                    \
  asm(PRE "v_mfma_f32_16x16x32_bf16 %0, %3, %6, %0\n\t"                                        \
      "v_mfma_f32_16x16x32_bf16 %1, %4, %6, %1\n\t"                                            \
      "v_mfma_f32_16x16x32_bf16 %2, %5, %6, %2"                                                \
      : "+v"(c0), "+v"(c1), "+v"(c2) : "v"(a0), "v"(a1), "v"(a2), "v"(b))

#define MPAR(a, par) ((((a) >> 13) << 14) | ((par) << 13) | ((a) & 8191u))

DI void chain_role(int h, const char* lds, unsigned ldsb, rsrc_t ws, const void* mem0, const void* Wq, const void* Wk,
                   const void* Wv, const void* Wo) {
  char* L = (char*)lds;
  const int t = threadIdx.x, lane = t & 63;
  const int w = __builtin_amdgcn_readfirstlane(t >> 6);
  const int gb = lane >> 4, qq = (lane >> 2) & 3, pp = lane & 3;
  const unsigned parP = 0, parM = 0;
  unsigned spins = 0;

  {
    rsrc_t rm = mkrsrc(mem0, 8192);
#pragma unroll
    for (int i = 0; i < 2; i++) {
      unsigned o = w * 2048 + i * 1024 + lane * 16;
      v4u v = ldp16(rm, ((o >> 4) & 7) * 1024 + (o >> 7) * 16);
      *(v4u*)(L + CH_MEM + o) = v;
    }
    wait_vm<0>();
  }
  v8u wq[2][8], wk[2][8], wo[8][2];
  {
    const v4u sq = mksrd(Wq, 512 * 4096 * 2), sk = mksrd(Wk, 512 * 4096 * 2), sv = mksrd(Wv, 512 * 4096 * 2),
              so = mksrd(Wo, 4096 * 512 * 2);
    unsigned vo[4];
#pragma unroll
    for (int i = 0; i < 4; i++) {
      unsigned f = (gb & 1) | ((i & 1) << 1) | ((i >> 1) << 2);
      vo[i] = gb * 8192 + (((((lane & 15) >> 1) ^ f) << 1) | (lane & 1)) * 16;
    }
    unsigned vw[4];
#pragma unroll
    for (int i = 0; i < 4; i++) {
      unsigned f = i | (((w >> 1) & 1) << 2);
      vw[i] = ((((lane >> 1) ^ f) << 1) | (lane & 1)) * 16;
    }
    auto issue = [&](int c) {
      const int mat = c >> 2, cc = c & 3;
      if (mat != 2) {
        const v4u srd = mat == 0 ? sq : (mat == 1 ? sk : sv);
#pragma unroll
        for (int i = 0; i < 8; i++) {
          unsigned row = 128 * cc + 32 * w + 4 * i;
          dma16(srd, vo[i & 3], row * 8192 + h * 256, ldsb + CH_WV + row * 256);
        }
      } else {
#pragma unroll
        for (int i = 0; i < 8; i++) {
          unsigned e = 32 * cc + 8 * w + i;
          dma16(so, vw[(i & 1) | (((i >> 2) & 1) << 1)], (h * 128 + e) * 1024, ldsb + CH_WV + cc * 32768 + (8 * w + i) * 1024);
        }
      }
    };
    const unsigned fq = (qq & 1) | ((gb & 1) << 1) | ((qq >> 1) << 2);
    const unsigned bq = CH_WV + (16 * (gb >> 1) + 4 * (gb & 1) + 8 * (qq >> 1) + (qq & 1)) * 256 + pp * 8;
    const unsigned bq0 = bq + (((2 * w) ^ fq) * 32), bq1 = bq + (((2 * w + 1) ^ fq) * 32);
    const unsigned fo = (qq & 1) | ((qq >> 1) << 1) | ((gb & 1) << 2);
    const unsigned bo = CH_WV + (16 * (gb & 1) + 8 * (gb >> 1) + 4 * (qq >> 1) + (qq & 1)) * 1024 + pp * 8;
#pragma unroll
    for (int c = 0; c < 4; c++) issue(c);
#pragma unroll
    for (int c = 0; c < 12; c++) {
      wait_vm<24>();
      bar();
      const int mat = c >> 2, cc = c & 3;
      if (mat < 2) {
#pragma unroll
        for (int tl = 0; tl < 2; tl++)
#pragma unroll
          for (int kl = 0; kl < 2; kl++) {
            const int kb = 2 * cc + kl;
            v2u r[4];
#pragma unroll
            for (int m = 0; m < 4; m++) r[m] = trd(L, (tl ? bq1 : bq0) + (64 * kb + 32 * (m >> 1) + 2 * (m & 1)) * 256);
            v8u x = {r[0].x, r[0].y, r[1].x, r[1].y, r[2].x, r[2].y, r[3].x, r[3].y};
            asm volatile("" : "+a"(x));
            if (mat == 0) wq[tl][kb] = x; else wk[tl][kb] = x;
          }
      } else {
#pragma unroll
        for (int tl = 0; tl < 8; tl++) {
#pragma unroll
          for (int ml = 0; ml < 2; ml++) {
            v2u r = trd(L, cc * 32768 + bo + 2 * ml * 1024 + (((8 * w + tl) ^ fo) * 32));
            wo[tl][cc >> 1][(cc & 1) * 4 + ml * 2] = r.x;
            wo[tl][cc >> 1][(cc & 1) * 4 + ml * 2 + 1] = r.y;
          }
        }
      }
      bar();
      issue(c + 4);
    }
  }
  const unsigned idx = (lane & 8) ? 0xEEEEu : 0x4444u;
  const unsigned fq = (qq & 1) | ((gb & 1) << 1) | ((qq >> 1) << 2);
  const unsigned bq = (16 * (gb >> 1) + 4 * (gb & 1) + 8 * (qq >> 1) + (qq & 1)) * 256 + pp * 8;
  const unsigned bv0 = CH_WV + bq + (((2 * w) ^ fq) * 32), bv1 = CH_WV + bq + (((2 * w + 1) ^ fq) * 32);

  v4u vcur[2], vnxt[2], kpre[2];
  int kvknown;
  {
    int a = wait_flag(ws, FLG_KV + h * 128, 0, spins);
    int b = wait_flag(ws, FLG_KV + (32 + h) * 128, 0, spins);
    kvknown = min(a, b);
#pragma unroll
    for (int i = 0; i < 2; i++) {
      kpre[i] = ld16(ws, OFF_KX + h * (NSEG * 8192) + (2 * w + i) * 1024 + lane * 16);
      vcur[i] = ld16(ws, OFF_VX + h * (NSEG * 8192) + (2 * w + i) * 1024 + lane * 16);
    }
#pragma unroll
    for (int i = 0; i < 2; i++) *(v4u*)(L + CH_KX + (2 * w + i) * 1024 + lane * 16) = kpre[i];
  }
  wait_vm<0>();
  bar();

  const int pcol = lane & 15, phf = (lane >> 4) & 1;
  const unsigned pbase = (((8 * w + (lane >> 5)) * 4 + 2 * (pcol >> 3) + phf) * 32 + h) * 128 + (pcol & 7) * 16;
  const unsigned ibase = ((h * 4 + w) * 32) * 128 + lane * 16;
  const unsigned mpiece = (2 * h + (w >> 1)) * 128 + (4 * (w & 1) + (lane >> 4)) * 16;
  const v4u pois = {POIS, POIS, POIS, POIS};
  const unsigned xw = (32 * w + 16 * (lane >> 5) + (lane & 15)) * 16 + ((lane >> 4) & 1) * 8;
  const unsigned xr = (4 * gb + qq) * 16 + (pp & 1) * 8;

  int ms3 = 1;
  for (int s = 0;; s++) {
    if (s > 0) {
#pragma unroll
      for (int i = 0; i < 4; i++) st16(ws, OFF_INB + MPAR(((s + 1) & 1) * 524288 + ibase + i * 1024, parP), pois);
      int ps = ms3 + 1; if (ps == 3) ps = 0;
      if ((lane & 15) == 0) st16(ws, OFF_MEM + MPAR(ps * 8192 + mpiece, parM), pois);
      if (t == 0) st4(ws, FLG_M + h * 128, s - 1);
    }
    if (s + 1 < NSEG - 1) {
      if (kvknown < s + 1) {
        int a = wait_flag(ws, FLG_KV + h * 128, s + 1, spins);
        int b = wait_flag(ws, FLG_KV + (32 + h) * 128, s + 1, spins);
        kvknown = min(a, b);
      }
#pragma unroll
      for (int i = 0; i < 2; i++) {
        kpre[i] = ld16(ws, OFF_KX + (h * NSEG + s + 1) * 8192 + (2 * w + i) * 1024 + lane * 16);
        vnxt[i] = ld16(ws, OFF_VX + (h * NSEG + s + 1) * 8192 + (2 * w + i) * 1024 + lane * 16);
      }
    }
    f32x4 q0 = {0, 0, 0, 0}, q1 = q0, k0 = q0, k1 = q0;
#pragma unroll
    for (int kb = 0; kb < 8; kb++) {
      v4u a = *(const v4u*)(L + CH_MEM + kb * 1024 + lane * 16);
      if (kb == 0) SMF4(q0, q1, k0, k1, a, wq[0][kb], wq[1][kb], wk[0][kb], wk[1][kb], idx, "s_nop 1\n\t");
      else SMF4(q0, q1, k0, k1, a, wq[0][kb], wq[1][kb], wk[0][kb], wk[1][kb], idx, "");
    }
    MFENCE4(q0, q1, k0, k1);
    {
      float a0 = swapadd32(q0[0], q1[0]), a1 = swapadd32(q0[1], q1[1]), a2 = swapadd32(q0[2], q1[2]), a3 = swapadd32(q0[3], q1[3]);
      v2u u = {pk(a0, a1), pk(a2, a3)};
      *(v2u*)(L + CH_QT + xw) = u;
      float b0 = swapadd32(k0[0], k1[0]), b1 = swapadd32(k0[1], k1[1]), b2 = swapadd32(k0[2], k1[2]), b3 = swapadd32(k0[3], k1[3]);
      v2u v = {pk(b0, b1), pk(b2, b3)};
      *(v2u*)(L + CH_KT + xw) = v;
    }
    v2u vm;
    {
      f32x4 v0 = {0, 0, 0, 0}, v1 = v0;
#pragma unroll
      for (int kb = 0; kb < 8; kb++) {
        v4u a = *(const v4u*)(L + CH_MEM + kb * 1024 + lane * 16);
        v2u r0[4], r1[4];
#pragma unroll
        for (int m = 0; m < 4; m++) {
          r0[m] = trd(L, bv0 + (64 * kb + 32 * (m >> 1) + 2 * (m & 1)) * 256);
          r1[m] = trd(L, bv1 + (64 * kb + 32 * (m >> 1) + 2 * (m & 1)) * 256);
        }
        v8u b0 = {r0[0].x, r0[0].y, r0[1].x, r0[1].y, r0[2].x, r0[2].y, r0[3].x, r0[3].y};
        v8u b1 = {r1[0].x, r1[0].y, r1[1].x, r1[1].y, r1[2].x, r1[2].y, r1[3].x, r1[3].y};
        SMF2(v0, v1, a, b0, b1, idx, "s_nop 1\n\t");
      }
      MFENCE2(v0, v1);
      float a0 = swapadd32(v0[0], v1[0]), a1 = swapadd32(v0[1], v1[1]), a2 = swapadd32(v0[2], v1[2]), a3 = swapadd32(v0[3], v1[3]);
      vm.x = pk(a0, a1); vm.y = pk(a2, a3);
    }
    st8(ws, OFF_VM + (h * NSEG + s) * 2048 + w * 512 + lane * 8, vm);
    bar();
    v4u qf[4], km[4];
#pragma unroll
    for (int kk = 0; kk < 4; kk++) {
      v2u a = trd(L, CH_KT + xr + kk * 512), b = trd(L, CH_KT + xr + kk * 512 + 256);
      km[kk] = v4u{a.x, a.y, b.x, b.y};
    }
    {
      v4u kw = w == 0 ? km[0] : (w == 1 ? km[1] : (w == 2 ? km[2] : km[3]));
      if ((lane & 8) == 0) st16(ws, OFF_KM + (h * NSEG + s) * 2048 + (w * 4 + gb) * 128 + (lane & 7) * 16, kw);
    }
    if (s == NSEG - 1) break;
#pragma unroll
    for (int kk = 0; kk < 4; kk++) {
      v2u a = trd(L, CH_QT + xr + kk * 512), b = trd(L, CH_QT + xr + kk * 512 + 256);
      qf[kk] = v4u{a.x, a.y, b.x, b.y};
    }
    f32x4 sm = {0, 0, 0, 0}, s0 = sm, s1 = sm;
#pragma unroll
    for (int kk = 0; kk < 4; kk++) {
      v4u x0 = *(const v4u*)(L + CH_KX + kk * 1024 + lane * 16);
      v4u x1 = *(const v4u*)(L + CH_KX + (4 + kk) * 1024 + lane * 16);
      MF3(sm, s0, s1, km[kk], x0, x1, qf[kk], "s_nop 1\n\t");
    }
    MFENCE3(sm, s0, s1);
    float p[12];
#pragma unroll
    for (int i = 0; i < 4; i++) {
      p[i] = rbf(rbf(sm[i]) * SCALE);
      p[4 + i] = rbf(rbf(s0[i]) * SCALE);
      p[8 + i] = rbf(rbf(s1[i]) * SCALE);
    }
    float mx = fmaxf(fmaxf(fmaxf(p[0], p[1]), fmaxf(p[2], p[3])), fmaxf(fmaxf(fmaxf(p[4], p[5]), fmaxf(p[6], p[7])),
                                                                       fmaxf(fmaxf(p[8], p[9]), fmaxf(p[10], p[11]))));
    mx = xmax32(xmax16(mx));
    const float mb = mx * LOG2E;
#pragma unroll
    for (int i = 0; i < 12; i++) p[i] = __builtin_amdgcn_exp2f(fmaf(p[i], LOG2E, -mb));
    const bool lo = lane < 32;
    float sum = (lo ? (p[0] + p[1]) + (p[2] + p[3]) : 0.f) + ((p[4] + p[5]) + (p[6] + p[7])) + ((p[8] + p[9]) + (p[10] + p[11]));
    sum = xsum32(xsum16(sum));
    const float rs = __builtin_amdgcn_rcpf(sum);
    unsigned pm0 = pk(p[0], p[1]), pm1 = pk(p[2], p[3]);
    v2u pa = {lo ? pm0 : 0u, lo ? pm1 : 0u}, pb = {lo ? 0u : pm0, lo ? 0u : pm1};
    v4u px = {pk(p[4], p[5]), pk(p[6], p[7]), pk(p[8], p[9]), pk(p[10], p[11])};
    f32x4 oa, ob;
    asm("s_nop 1\n\t"
        "v_mfma_f32_16x16x16_bf16 %0, %2, %3, 0\n\t"
        "v_mfma_f32_16x16x16_bf16 %1, %2, %4, 0\n\t"
        "v_mfma_f32_16x16x32_bf16 %0, %5, %7, %0\n\t"
        "v_mfma_f32_16x16x32_bf16 %1, %6, %7, %1\n\t"
        "s_nop 7"
        : "=&v"(oa), "=&v"(ob) : "v"(vm), "v"(pa), "v"(pb), "v"(vcur[0]), "v"(vcur[1]), "v"(px));
    {
      v4u o = {pk(oa[0] * rs, oa[1] * rs), pk(oa[2] * rs, oa[3] * rs), pk(ob[0] * rs, ob[1] * rs), pk(ob[2] * rs, ob[3] * rs)};
      if ((lane & 8) == 0) *(v4u*)(L + CH_OL + (4 * w + gb) * 128 + (lane & 7) * 16) = o;
    }
    bar();
    if (s + 1 < NSEG - 1) {
#pragma unroll
      for (int i = 0; i < 2; i++) { *(v4u*)(L + CH_KX + (2 * w + i) * 1024 + lane * 16) = kpre[i]; vcur[i] = vnxt[i]; }
    }
    {
      v4u a0 = *(const v4u*)(L + CH_OL + lane * 16), a1 = *(const v4u*)(L + CH_OL + 1024 + lane * 16);
#pragma unroll
      for (int i = 0; i < 4; i++) {
        f32x4 c0 = {0, 0, 0, 0}, c1 = c0;
        SMFO(c0, c1, a0, a1, wo[2 * i][0], wo[2 * i][1], wo[2 * i + 1][0], wo[2 * i + 1][1], idx);
        MFENCE2(c0, c1);
        f32x4 r = {swapadd32(c0[0], c1[0]), swapadd32(c0[1], c1[1]), swapadd32(c0[2], c1[2]), swapadd32(c0[3], c1[3])};
        st16(ws, OFF_INB + MPAR((s & 1) * 524288 + pbase + i * 2 * 4 * 32 * 128, parP), __builtin_bit_cast(v4u, r));
      }
    }
    v4u g[4];
    while (true) {
      asm volatile("" ::: "memory");
#pragma unroll
      for (int i = 0; i < 4; i++) g[i] = ld16(ws, OFF_INB + MPAR((s & 1) * 524288 + ibase + i * 1024, parP));
      unsigned m = max(max(umax4(g[0]), umax4(g[1])), max(umax4(g[2]), umax4(g[3])));
      if (__all(m != POIS)) break;
      if (++spins > SPIN_LIMIT) break;
    }
    {
      f32x4 f0 = __builtin_bit_cast(f32x4, g[0]), f1 = __builtin_bit_cast(f32x4, g[1]), f2 = __builtin_bit_cast(f32x4, g[2]),
            f3 = __builtin_bit_cast(f32x4, g[3]);
      f32x4 su = (f0 + f1) + (f2 + f3);
      float a0 = swapadd32(su[0], su[2]), a1 = swapadd32(su[1], su[3]);
      auto s3 = __builtin_amdgcn_permlane16_swap(__float_as_uint(a0), __float_as_uint(a1), false, false);
      float c = __uint_as_float(s3[0]) + __uint_as_float(s3[1]);
      c += __uint_as_float(__builtin_amdgcn_update_dpp(0, __float_as_uint(c), 0x128  , 0xf, 0xf, false));
      float n = __uint_as_float(__builtin_amdgcn_update_dpp(0, __float_as_uint(c), 0xb1  , 0xf, 0xf, false));
      unsigned pr = pk(c, n);
      unsigned d0 = __builtin_amdgcn_update_dpp(0, pr, 0x00  , 0xf, 0xf, false);
      unsigned d1 = __builtin_amdgcn_update_dpp(0, pr, 0xaa  , 0xf, 0xf, false);
      unsigned d2 = __builtin_amdgcn_update_dpp(0, d0, 0x104  , 0xf, 0xf, false);
      unsigned d3 = __builtin_amdgcn_update_dpp(0, d1, 0x104, 0xf, 0xf, false);
      v4u o = {min(d0, POIS - 1), min(d1, POIS - 1), min(d2, POIS - 1), min(d3, POIS - 1)};
      if ((lane & 15) == 0) st16(ws, OFF_MEM + MPAR(ms3 * 8192 + mpiece, parM), o);
    }
    {
      v4u m0, m1;
      while (true) {
        asm volatile("" ::: "memory");
        m0 = ld16(ws, OFF_MEM + MPAR(ms3 * 8192 + (w * 2 + 0) * 1024 + lane * 16, parM));
        m1 = ld16(ws, OFF_MEM + MPAR(ms3 * 8192 + (w * 2 + 1) * 1024 + lane * 16, parM));
        if (__all(max(umax4(m0), umax4(m1)) != POIS)) break;
        if (++spins > SPIN_LIMIT) break;
      }
      *(v4u*)(L + CH_MEM + w * 2048 + lane * 16) = m0;
      *(v4u*)(L + CH_MEM + w * 2048 + 1024 + lane * 16) = m1;
    }
    wait_vm<0>();
    bar();
    ms3 = ms3 == 2 ? 0 : ms3 + 1;
  }
  wait_vm<0>();
  bar();
  if (t == 0) st4(ws, FLG_M + h * 128, NSEG - 1);
}

constexpr unsigned XB0 = 131072, XB1 = 0;
DI void load_w_frags(v4u (&wf)[2][16], const void* W, int h, int w, int lane, char* L, unsigned ldsb) {
  const int gb = lane >> 4, qq = (lane >> 2) & 3, pp = lane & 3;
  const v4u srd = mksrd(W, 512 * 4096 * 2);
  unsigned vo[2];
#pragma unroll
  for (int b = 0; b < 2; b++) {
    unsigned f = gb | (b << 2);
    vo[b] = gb * 8192 + (((((lane & 15) >> 1) ^ f) << 1) | (lane & 1)) * 16;
  }
#pragma unroll
  for (int cc = 0; cc < 4; cc++)
#pragma unroll
    for (int i = 0; i < 8; i++) {
      unsigned row = 128 * cc + 32 * w + 4 * i;
      dma16(srd, vo[(i >> 1) & 1], row * 8192 + h * 256, ldsb + row * 256);
    }
  wait_vm<0>();
  bar();
  const unsigned f = qq | ((gb & 1) << 2);
  const unsigned b = (8 * gb + qq) * 256 + pp * 8;
#pragma unroll
  for (int tl = 0; tl < 2; tl++) {
    const unsigned bt = b + (((2 * w + tl) ^ f) * 32);
#pragma unroll
    for (int ks = 0; ks < 16; ks++) {
      v2u a = trd(L, bt + (32 * ks) * 256), c = trd(L, bt + (32 * ks + 4) * 256);
      wf[tl][ks] = v4u{a.x, a.y, c.x, c.y};
    }
  }
  bar();
}
DI void issue_x(v4u srdx, int s, int w, int lane, unsigned ldsb, unsigned xb) {
#pragma unroll
  for (int i = 0; i < 8; i++) {
    unsigned row = 8 * w + i;
    dma16(srdx, (lane ^ (row & 15)) * 16, (s * 32 + row) * 1024, ldsb + xb + row * 1024);
  }
}
DI v4u read_x(const char* L, unsigned xb, int tt, int ks, int lane) {
  const int r = lane & 15, g = lane >> 4;
  return *(const v4u*)(L + xb + (16 * tt + r) * 1024 + (((4 * ks + g) ^ r) * 16));
}

template <int TYPE>
DI void kv_role(int h, char* L, unsigned ldsb, rsrc_t ws, const void* x, const void* W) {
  const int t = threadIdx.x, lane = t & 63;
  const int w = __builtin_amdgcn_readfirstlane(t >> 6);
  const v4u srdx = mksrd(x, 2048 * 512 * 2);
  issue_x(srdx, 0, w, lane, ldsb, XB0);
  v4u wf[2][16];
  load_w_frags(wf, W, h, w, lane, L, ldsb);
  for (int s = 0; s < NSEG; s++) {
    const unsigned cur = (s & 1) ? XB1 : XB0;
    if (s + 1 < NSEG) issue_x(srdx, s + 1, w, lane, ldsb, (s & 1) ? XB0 : XB1);
    f32x4 acc[2][2] = {};
#pragma unroll
    for (int ks = 0; ks < 16; ks++) {
      v4u x0 = read_x(L, cur, 0, ks, lane), x1 = read_x(L, cur, 1, ks, lane);
#pragma unroll
      for (int tl = 0; tl < 2; tl++) {
        if (TYPE == 0) { acc[tl][0] = mfma16(wf[tl][ks], x0, acc[tl][0]); acc[tl][1] = mfma16(wf[tl][ks], x1, acc[tl][1]); }
        else { acc[tl][0] = mfma16(x0, wf[tl][ks], acc[tl][0]); acc[tl][1] = mfma16(x1, wf[tl][ks], acc[tl][1]); }
      }
    }
    if (TYPE == 0) {
#pragma unroll
      for (int tt = 0; tt < 2; tt++) {
        v4u o = {pk(acc[0][tt][0], acc[0][tt][1]), pk(acc[0][tt][2], acc[0][tt][3]), pk(acc[1][tt][0], acc[1][tt][1]),
                 pk(acc[1][tt][2], acc[1][tt][3])};
        st16(ws, OFF_KX + (h * NSEG + s) * 8192 + (tt * 4 + w) * 1024 + lane * 16, o);
      }
    } else {
#pragma unroll
      for (int tl = 0; tl < 2; tl++) {
        v4u o = {pk(acc[tl][0][0], acc[tl][0][1]), pk(acc[tl][0][2], acc[tl][0][3]), pk(acc[tl][1][0], acc[tl][1][1]),
                 pk(acc[tl][1][2], acc[tl][1][3])};
        st16(ws, OFF_VX + (h * NSEG + s) * 8192 + (2 * w + tl) * 1024 + lane * 16, o);
      }
    }
    wait_vm<0>();
    bar();
    if (t == 0) st4(ws, FLG_KV + (TYPE * 32 + h) * 128, s);
  }
}

constexpr unsigned QA_QX = 32768;
DI void qa_role(int h, char* L, unsigned ldsb, rsrc_t ws, const void* x, const void* Wq) {
  const int t = threadIdx.x, lane = t & 63;
  const int w = __builtin_amdgcn_readfirstlane(t >> 6);
  const int mh = w & 1, eh = w >> 1, g = lane >> 4, c = lane & 15;
  const v4u srdx = mksrd(x, 2048 * 512 * 2);
  unsigned spins = 0;
  issue_x(srdx, 0, w, lane, ldsb, XB0);
  v4u wf[2][16];
  load_w_frags(wf, Wq, h, w, lane, L, ldsb);
  int kvknown = -1;
  for (int s = 0; s < NSEG; s++) {
    const unsigned cur = (s & 1) ? XB1 : XB0;
    if (s + 1 < NSEG) issue_x(srdx, s + 1, w, lane, ldsb, (s & 1) ? XB0 : XB1);
    {
      f32x4 acc[2][2] = {};
#pragma unroll
      for (int ks = 0; ks < 16; ks++) {
        v4u x0 = read_x(L, cur, 0, ks, lane), x1 = read_x(L, cur, 1, ks, lane);
#pragma unroll
        for (int tl = 0; tl < 2; tl++) {
          acc[tl][0] = mfma16(wf[tl][ks], x0, acc[tl][0]);
          acc[tl][1] = mfma16(wf[tl][ks], x1, acc[tl][1]);
        }
      }
#pragma unroll
      for (int tt = 0; tt < 2; tt++) {
        v4u o = {pk(acc[0][tt][0], acc[0][tt][1]), pk(acc[0][tt][2], acc[0][tt][3]), pk(acc[1][tt][0], acc[1][tt][1]),
                 pk(acc[1][tt][2], acc[1][tt][3])};
        *(v4u*)(L + QA_QX + (tt * 4 + w) * 1024 + lane * 16) = o;
      }
    }
    bar();
    v4u qf[4];
#pragma unroll
    for (int kk = 0; kk < 4; kk++) qf[kk] = *(const v4u*)(L + QA_QX + (mh * 4 + kk) * 1024 + lane * 16);
    if (kvknown < s) {
      int a = wait_flag(ws, FLG_KV + h * 128, s, spins);
      int b = wait_flag(ws, FLG_KV + (32 + h) * 128, s, spins);
      kvknown = min(a, b);
    }
    v4u kx[2][4], vx[4];
#pragma unroll
    for (int i = 0; i < 8; i++) kx[i >> 2][i & 3] = ld16(ws, OFF_KX + (h * NSEG + s) * 8192 + i * 1024 + lane * 16);
#pragma unroll
    for (int i = 0; i < 4; i++) vx[i] = ld16(ws, OFF_VX + (h * NSEG + s) * 8192 + (4 * eh + i) * 1024 + lane * 16);
    wait_flag(ws, FLG_M + h * 128, s, spins);
    v4u km[4];
    v2u vm[2];
#pragma unroll
    for (int kk = 0; kk < 4; kk++) km[kk] = ld16(ws, OFF_KM + (h * NSEG + s) * 2048 + (kk * 4 + g) * 128 + (lane & 7) * 16);
#pragma unroll
    for (int i = 0; i < 2; i++) vm[i] = ld8(ws, OFF_VM + (h * NSEG + s) * 2048 + (2 * eh + i) * 512 + lane * 8);
    f32x4 sm = {0, 0, 0, 0}, s0 = sm, s1 = sm;
#pragma unroll
    for (int kk = 0; kk < 4; kk++) {
      s0 = mfma16(kx[0][kk], qf[kk], s0);
      s1 = mfma16(kx[1][kk], qf[kk], s1);
      sm = mfma16(km[kk], qf[kk], sm);
    }
    float p[12];
    const int tok = 16 * mh + c;
#pragma unroll
    for (int i = 0; i < 4; i++) {
      p[i] = rbf(rbf(sm[i]) * SCALE);
      p[4 + i] = (4 * g + i <= tok) ? rbf(rbf(s0[i]) * SCALE) : NEG_MASK;
      p[8 + i] = (16 + 4 * g + i <= tok) ? rbf(rbf(s1[i]) * SCALE) : NEG_MASK;
    }
    float mx = fmaxf(fmaxf(fmaxf(p[0], p[1]), fmaxf(p[2], p[3])), fmaxf(fmaxf(fmaxf(p[4], p[5]), fmaxf(p[6], p[7])),
                                                                       fmaxf(fmaxf(p[8], p[9]), fmaxf(p[10], p[11]))));
    mx = xmax32(xmax16(mx));
#pragma unroll
    for (int i = 0; i < 12; i++) p[i] = __builtin_amdgcn_exp2f((p[i] - mx) * LOG2E);
    const bool lo = lane < 32;
    float sum = (lo ? (p[0] + p[1]) + (p[2] + p[3]) : 0.f) + ((p[4] + p[5]) + (p[6] + p[7])) + ((p[8] + p[9]) + (p[10] + p[11]));
    sum = xsum32(xsum16(sum));
    const float rs = 1.0f / sum;
    unsigned pm0 = pk(p[0] * rs, p[1] * rs), pm1 = pk(p[2] * rs, p[3] * rs);
    v2u pa = {lo ? pm0 : 0u, lo ? pm1 : 0u}, pb = {lo ? 0u : pm0, lo ? 0u : pm1};
    v4u px = {pk(p[4] * rs, p[5] * rs), pk(p[6] * rs, p[7] * rs), pk(p[8] * rs, p[9] * rs), pk(p[10] * rs, p[11] * rs)};
    f32x4 o[4];
#pragma unroll
    for (int i = 0; i < 4; i++) {
      f32x4 z = {0, 0, 0, 0};
      z = __builtin_amdgcn_mfma_f32_16x16x16bf16_1k(__builtin_bit_cast(s16x4, vm[i >> 1]), __builtin_bit_cast(s16x4, (i & 1) ? pb : pa), z, 0, 0, 0);
      o[i] = mfma16(vx[i], px, z);
    }
#pragma unroll
    for (int i = 0; i < 2; i++) {
      v4u u = {pk(o[2 * i][0], o[2 * i][1]), pk(o[2 * i][2], o[2 * i][3]), pk(o[2 * i + 1][0], o[2 * i + 1][1]),
               pk(o[2 * i + 1][2], o[2 * i + 1][3])};
      st16(ws, OFF_OX + (s * 32 + h) * 8192 + (mh * 4 + 2 * eh + i) * 1024 + lane * 16, u);
    }
    wait_vm<0>();
    bar();
    if (t == 0) st4(ws, FLG_O + h * 4, s);
  }
}

DI void wo_role(int par, int mh, int ng, char* L, unsigned ldsb, rsrc_t ws, const void* Wo, void* out) {
  const int t = threadIdx.x, lane = t & 63;
  const int w = __builtin_amdgcn_readfirstlane(t >> 6);
  const int g = lane >> 4, qq = (lane >> 2) & 3, pp = lane & 3;
  unsigned spins = 0;
  v4u wo[32][2];
  {
    const v4u srd = mksrd(Wo, 4096 * 512 * 2);
    const unsigned vo = (lane >> 2) * 1024 + (lane & 3) * 16;
    const unsigned lb = w * 32768;
#pragma unroll
    for (int rd = 0; rd < 2; rd++) {
#pragma unroll
      for (int i = 0; i < 32; i++)
        dma16(srd, vo, ((8 * w + 4 * rd) * 128 + 16 * i) * 1024 + ng * 64, ldsb + lb + i * 1024);
      wait_vm<0>();
#pragma unroll
      for (int hl = 0; hl < 4; hl++)
#pragma unroll
        for (int kk = 0; kk < 4; kk++)
#pragma unroll
          for (int ct = 0; ct < 2; ct++) {
            unsigned b = lb + (hl * 128 + 32 * kk + 4 * g + qq) * 64 + pp * 16 + ct * 8;
            v2u a = trd(L, b), c = trd(L, b + 16 * 64);
            wo[(rd * 4 + hl) * 4 + kk][ct] = v4u{a.x, a.y, c.x, c.y};
          }
      asm volatile("s_waitcnt lgkmcnt(0)" ::: "memory");
    }
  }
  bar();
  rsrc_t ro = mkrsrc(out, 2048 * 1024 * 2);
  int it = 0;
  for (int s = par; s < NSEG; s += 2, it ^= 1) {
    while (true) {
      asm volatile("" ::: "memory");
      int v = (int)ld4(ws, FLG_O + (lane & 31) * 4);
      if (__all(v >= s)) break;
      if (++spins > SPIN_LIMIT) break;
      __builtin_amdgcn_s_sleep(4);
    }
    f32x4 a0 = {0, 0, 0, 0}, a1 = a0;
#pragma unroll
    for (int b = 0; b < 2; b++) {
      v4u of[16];
#pragma unroll
      for (int i = 0; i < 16; i++) {
        const int ks = b * 16 + i;
        of[i] = ld16(ws, OFF_OX + (s * 32 + 8 * w + (ks >> 2)) * 8192 + (mh * 4 + (ks & 3)) * 1024 + lane * 16);
      }
#pragma unroll
      for (int i = 0; i < 16; i++) {
        a0 = mfma16(wo[b * 16 + i][0], of[i], a0);
        a1 = mfma16(wo[b * 16 + i][1], of[i], a1);
      }
    }
    char* R = L + it * 8192;
    *(f32x4*)(R + w * 2048 + lane * 16) = a0;
    *(f32x4*)(R + w * 2048 + 1024 + lane * 16) = a1;
    bar();
    if (w == 0) {
      f32x4 r0 = {0, 0, 0, 0}, r1 = r0;
#pragma unroll
      for (int i = 0; i < 4; i++) {
        r0 += *(const f32x4*)(R + i * 2048 + lane * 16);
        r1 += *(const f32x4*)(R + i * 2048 + 1024 + lane * 16);
      }
      v4u o = {pk(r0[0], r0[1]), pk(r0[2], r0[3]), pk(r1[0], r1[1]), pk(r1[2], r1[3])};
      stp16(ro, (s * 32 + 16 * mh + (lane & 15)) * 2048 + 1024 + ng * 64 + g * 16, o);
    }
  }
}

constexpr unsigned CV_KV = 0, CV_CH = 5200, CV_Y = 41600, CV_H = 83200, CV_HS = 4160;
DI void cv_role(int cv, char* L, const void* q, const void* k, const void* v, const void* shk, const void* ssk,
                const void* ssd, void* out) {
  const int t = threadIdx.x, lane = t & 63;
  const int w = __builtin_amdgcn_readfirstlane(t >> 6);
  const int d0 = cv * 8;
  rsrc_t rq = mkrsrc(q, 2048 * 512 * 2), rk = mkrsrc(k, 2048 * 512 * 2), rv = mkrsrc(v, 2048 * 512 * 2);
  {
    rsrc_t rs = mkrsrc(shk, 512 * 4 * 2);
    v4u sh[4];
#pragma unroll
    for (int i = 0; i < 4; i++) sh[i] = ldp16(rs, d0 * 8 + i * 16);
    v4u kr[11], vr[8];
#pragma unroll
    for (int i = 0; i < 11; i++) {
      int s = 8 * t + i - 3;
      kr[i] = ldp16(rk, s < 0 ? 0x80000000u : (unsigned)(s * 1024 + d0 * 2));
    }
#pragma unroll
    for (int i = 0; i < 8; i++) vr[i] = ldp16(rv, (8 * t + i) * 1024 + d0 * 2);
    if (t < 40) *(v4u*)(L + CV_KV + (t / 5) * CV_CH + (t % 5) * 16) = v4u{0, 0, 0, 0};
#pragma unroll
    for (int ch = 0; ch < 8; ch++) {
      float tap[4];
      {
        unsigned u0 = sh[ch >> 1][(ch & 1) * 2], u1 = sh[ch >> 1][(ch & 1) * 2 + 1];
        tap[0] = bflo(u0); tap[1] = bfhi(u0); tap[2] = bflo(u1); tap[3] = bfhi(u1);
      }
      float r[8];
#pragma unroll
      for (int i = 0; i < 8; i++) {
        float a = 0.f;
#pragma unroll
        for (int tt = 0; tt < 4; tt++) {
          unsigned u = kr[i + tt][ch >> 1];
          float kk = (ch & 1) ? bfhi(u) : bflo(u);
          a = rbf(a + rbf(kk * tap[3 - tt]));
        }
        unsigned uv = vr[i][ch >> 1];
        r[i] = a * ((ch & 1) ? bfhi(uv) : bflo(uv));
      }
      v4u o = {pk(r[0], r[1]), pk(r[2], r[3]), pk(r[4], r[5]), pk(r[6], r[7])};
      *(v4u*)(L + CV_KV + ch * CV_CH + (1 + (t >> 2)) * 80 + (t & 3) * 16) = o;
    }
  }
  bar();
  const int r = lane & 31, kg = lane >> 5;
  for (int cc = 0; cc < 2; cc++) {
    const int ch = 2 * w + cc;
    char* H = L + CV_H + w * (4 * CV_HS);
    char* HL = L + CV_Y + ch * CV_CH;
    {
      rsrc_t rh = mkrsrc(ssk, 512 * 2048 * 2);
#pragma unroll
      for (int i = 0; i < 4; i++) *(v4u*)(HL + i * 1024 + lane * 16) = ldp16(rh, (d0 + ch) * 4096 + i * 1024 + lane * 16);
      asm volatile("s_waitcnt lgkmcnt(0)" ::: "memory");
      for (int u = lane; u < 4 * 520; u += 64) {
        const int sg = u / 520, m = (u % 520) * 4;
        unsigned e[4];
#pragma unroll
        for (int j = 0; j < 4; j++) {
          int n = 2048 - m - j + sg;
          unsigned val = *(const unsigned short*)(HL + (n & 2047) * 2);
          e[j] = (n >= 0 && n < 2048) ? val : 0u;
        }
        *(v2u*)(H + sg * CV_HS + m * 2) = v2u{e[0] | (e[1] << 16), e[2] | (e[3] << 16)};
      }
      asm volatile("s_waitcnt lgkmcnt(0)" ::: "memory");
    }
    const char* U = L + CV_KV + ch * CV_CH;
    const unsigned ha = (r & 3) * CV_HS + (2048 - 4 * (r >> 2) + 8 * kg) * 2;
    f32x16 y0 = {}, y1 = {};
#pragma unroll 2
    for (int dl = 0; dl < 32; dl++) {
      const int bi = max(r - dl + 1, 0);
#pragma unroll
      for (int ks = 0; ks < 2; ks++) {
        const char* pa = H + ha - dl * 64 + ks * 32;
        v2u a00 = *(const v2u*)(pa), a01 = *(const v2u*)(pa + 8);
        v2u a10 = *(const v2u*)(pa - 2048), a11 = *(const v2u*)(pa - 2048 + 8);
        v4u b0 = *(const v4u*)(U + bi * 80 + ks * 32 + kg * 16);
        v4u b1 = *(const v4u*)(U + (r + 33 - dl) * 80 + ks * 32 + kg * 16);
        bf16x8 A0 = __builtin_bit_cast(bf16x8, v4u{a00.x, a00.y, a01.x, a01.y});
        bf16x8 A1 = __builtin_bit_cast(bf16x8, v4u{a10.x, a10.y, a11.x, a11.y});
        y0 = __builtin_amdgcn_mfma_f32_32x32x16_bf16(A0, __builtin_bit_cast(bf16x8, b0), y0, 0, 0, 0);
        y1 = __builtin_amdgcn_mfma_f32_32x32x16_bf16(A0, __builtin_bit_cast(bf16x8, b1), y1, 0, 0, 0);
        y1 = __builtin_amdgcn_mfma_f32_32x32x16_bf16(A1, __builtin_bit_cast(bf16x8, b0), y1, 0, 0, 0);
      }
    }
    char* Y = L + CV_Y + ch * CV_CH;
#pragma unroll
    for (int n = 0; n < 4; n++) {
      *(v2u*)(Y + (1 + r) * 80 + (8 * n + 4 * kg) * 2) = v2u{pk(y0[4 * n], y0[4 * n + 1]), pk(y0[4 * n + 2], y0[4 * n + 3])};
      *(v2u*)(Y + (33 + r) * 80 + (8 * n + 4 * kg) * 2) = v2u{pk(y1[4 * n], y1[4 * n + 1]), pk(y1[4 * n + 2], y1[4 * n + 3])};
    }
    asm volatile("s_waitcnt lgkmcnt(0)" ::: "memory");
  }
  bar();
  {
    rsrc_t rd = mkrsrc(ssd, 512 * 2), ro = mkrsrc(out, 2048 * 1024 * 2);
    v4u dv = ldp16(rd, d0 * 2);
    v4u qr[8];
#pragma unroll
    for (int i = 0; i < 8; i++) qr[i] = ldp16(rq, (8 * t + i) * 1024 + d0 * 2);
    float res[8][8];
#pragma unroll
    for (int ch = 0; ch < 8; ch++) {
      v4u yy = *(const v4u*)(L + CV_Y + ch * CV_CH + (1 + (t >> 2)) * 80 + (t & 3) * 16);
      v4u kk = *(const v4u*)(L + CV_KV + ch * CV_CH + (1 + (t >> 2)) * 80 + (t & 3) * 16);
      float D = (ch & 1) ? bfhi(dv[ch >> 1]) : bflo(dv[ch >> 1]);
#pragma unroll
      for (int i = 0; i < 8; i++) {
        float y = (i & 1) ? bfhi(yy[i >> 1]) : bflo(yy[i >> 1]);
        float kvv = (i & 1) ? bfhi(kk[i >> 1]) : bflo(kk[i >> 1]);
        float qv = (ch & 1) ? bfhi(qr[i][ch >> 1]) : bflo(qr[i][ch >> 1]);
        res[i][ch] = qv * rbf(y + rbf(kvv * D));
      }
    }
#pragma unroll
    for (int i = 0; i < 8; i++) {
      v4u o = {pk(res[i][0], res[i][1]), pk(res[i][2], res[i][3]), pk(res[i][4], res[i][5]), pk(res[i][6], res[i][7])};
      stp16(ro, (8 * t + i) * 2048 + d0 * 2, o);
    }
  }
}

__global__ void __launch_bounds__(256, 1)
fused_kernel(const void* Aq, const void* Ak, const void* Av, const void* Ashk, const void* Assk, const void* Assd,
             const void* Bx, const void* Bmem0, const void* Wq, const void* Wk, const void* Wv, const void* Wo, void* out,
             char* wsb) {
  __shared__ __attribute__((aligned(1024))) char lds[163840];
  const unsigned ldsb = (unsigned)(size_t)(lds_char_p)lds;
  rsrc_t ws = mkrsrc(wsb, WS_SPAN);
  const int b = blockIdx.x, xcd = b & 7, slot = b >> 3;
  if (xcd < 4) {
    if (slot < 8) chain_role(xcd * 8 + slot, lds, ldsb, ws, Bmem0, Wq, Wk, Wv, Wo);
    else if (slot < 16) cv_role(xcd * 8 + (slot - 8), lds, Aq, Ak, Av, Ashk, Assk, Assd, out);
    else {
      const int h = xcd * 8 + ((slot - 16) & 7);
      if (slot < 24) kv_role<0>(h, lds, ldsb, ws, Bx, Wk);
      else kv_role<1>(h, lds, ldsb, ws, Bx, Wv);
    }
  } else {
    if (slot < 16) wo_role((xcd - 4) >> 1, (xcd - 4) & 1, slot, lds, ldsb, ws, Wo, out);
    else if (slot < 24) cv_role(xcd * 8 + (slot - 16), lds, Aq, Ak, Av, Ashk, Assk, Assd, out);
    else qa_role((xcd - 4) * 8 + (slot - 24), lds, ldsb, ws, Bx, Wq);
  }
}

extern "C" void kernel_launch(void* const* d_in, const int* in_sizes, int n_in, void* d_out, int out_size, void* d_ws,
                              size_t ws_size, hipStream_t stream) {
  char* wsb = (char*)(((uintptr_t)d_ws + (2u << 20) - 1) & ~(uintptr_t)((2u << 20) - 1));
  (void)hipMemsetAsync(wsb, 0xff, INIT_BYTES, stream);
  fused_kernel<<<256, 256, 0, stream>>>(d_in[0], d_in[1], d_in[2], d_in[3], d_in[4], d_in[5], d_in[6], d_in[7], d_in[8],
                                        d_in[9], d_in[10], d_in[11], d_out, wsb);
}
```

```cpp
#define KX_EARLY 1
#define NDIR 4
#define CODE_PF_ID 1
#define FLG_ADAPT 1
#define WO_LONG 32
#define KV_DELAY 0
#define KV_LAZY 2
#define PF_WQ 6
#define PF_WO 3
#define MAPV 3
#define QA_LAZY 56
#include <hip/hip_runtime.h>
#include <stdint.h>

typedef unsigned v2u __attribute__((ext_vector_type(2)));
typedef unsigned v4u __attribute__((ext_vector_type(4)));
typedef unsigned v8u __attribute__((ext_vector_type(8)));
typedef float f32x4 __attribute__((ext_vector_type(4)));
typedef float f32x16 __attribute__((ext_vector_type(16)));
typedef __bf16 bf16x2 __attribute__((ext_vector_type(2)));
typedef __bf16 bf16x8 __attribute__((ext_vector_type(8)));
typedef short s16x4 __attribute__((ext_vector_type(4)));
typedef __attribute__((address_space(3))) s16x4* lds_s16x4_p;
typedef __attribute__((address_space(3))) char* lds_char_p;

#define DI __device__ __forceinline__

constexpr int NSEG = 64;
constexpr unsigned OFF_INB = 0;
constexpr unsigned OFF_MEM = 2u << 20;
constexpr unsigned OFF_FLG = OFF_MEM + 49152;
constexpr unsigned FLG_KV = OFF_FLG;
constexpr unsigned FLG_M = OFF_FLG + 8192;
constexpr unsigned FLG_O = OFF_FLG + 12288;
constexpr unsigned OFF_PAR = OFF_MEM + 65536;
constexpr unsigned OFF_XPH = OFF_PAR + 128;
constexpr unsigned OFF_KX = 4u << 20;
constexpr unsigned OFF_VX = 20u << 20;
constexpr unsigned OFF_OX = 36u << 20;
constexpr unsigned OFF_KM = 52u << 20;
constexpr unsigned OFF_VM = 56u << 20;
constexpr unsigned WS_SPAN = 60u << 20;
constexpr unsigned POIS = 0xffffffffu;
#define NPROBE 4
#define WO_SLEEP 4
#define QA_SLEEP 2
#define LAST_N 3
#define NFILL 168
#define H_ROT 0
#define PMODE 1
constexpr int NPB = PMODE ? 32 : 8;
#define PREF 0
#define PREF_N 4
static __device__ unsigned g_xph;
constexpr int AUX_SC1 = 16;
constexpr unsigned SPIN_LIMIT = 1u << 21;

typedef __amdgpu_buffer_rsrc_t rsrc_t;
DI rsrc_t mkrsrc(const void* p, unsigned bytes) { return __builtin_amdgcn_make_buffer_rsrc((void*)p, 0, bytes, 0x00020000); }
DI v4u mksrd(const void* p, unsigned bytes) {
  uint64_t a = (uint64_t)p;
  v4u r = {(unsigned)a, (unsigned)(a >> 32) & 0xffffu, bytes, 0x00020000u};
  return r;
}
DI v4u ld16(rsrc_t r, unsigned off) { return __builtin_amdgcn_raw_buffer_load_b128(r, off, 0, AUX_SC1); }
DI v2u ld8(rsrc_t r, unsigned off) { return __builtin_amdgcn_raw_buffer_load_b64(r, off, 0, AUX_SC1); }
DI unsigned ld4(rsrc_t r, unsigned off) { return __builtin_amdgcn_raw_buffer_load_b32(r, off, 0, AUX_SC1); }
DI void st16(rsrc_t r, unsigned off, v4u v) { __builtin_amdgcn_raw_buffer_store_b128(v, r, off, 0, AUX_SC1); }
DI void st8(rsrc_t r, unsigned off, v2u v) { __builtin_amdgcn_raw_buffer_store_b64(v, r, off, 0, AUX_SC1); }
DI void st4(rsrc_t r, unsigned off, unsigned v) { __builtin_amdgcn_raw_buffer_store_b32(v, r, off, 0, AUX_SC1); }
DI v4u ldp16(rsrc_t r, unsigned off) { return __builtin_amdgcn_raw_buffer_load_b128(r, off, 0, 0); }
DI void stp16(rsrc_t r, unsigned off, v4u v) { __builtin_amdgcn_raw_buffer_store_b128(v, r, off, 0, 0); }

DI void dma16(v4u srd, unsigned voff, unsigned soff, unsigned ldsaddr) {
  asm volatile("s_mov_b32 m0, %3\n\ts_nop 0\n\tbuffer_load_dwordx4 %0, %1, %2 offen lds"
               :: "v"(voff), "s"(srd), "s"(soff), "s"(ldsaddr) : "memory");
}
DI void bar() { asm volatile("s_waitcnt lgkmcnt(0)\n\ts_barrier" ::: "memory"); }
template <int N> DI void wait_vm() { asm volatile("s_waitcnt vmcnt(%0)" :: "i"(N) : "memory"); }
DI v2u trd(const char* lds, unsigned off) {
  return __builtin_bit_cast(v2u, __builtin_amdgcn_ds_read_tr16_b64_v4i16((lds_s16x4_p)(lds + off)));
}
DI unsigned pk(float a, float b) { bf16x2 v = {(__bf16)a, (__bf16)b}; return __builtin_bit_cast(unsigned, v); }
DI float bflo(unsigned u) { return __uint_as_float(u << 16); }
DI float bfhi(unsigned u) { return __uint_as_float(u & 0xffff0000u); }
DI float rbf(float a) { return bflo(pk(a, a)); }
DI f32x4 mfma16(v4u a, v4u b, f32x4 c) {
  return __builtin_amdgcn_mfma_f32_16x16x32_bf16(__builtin_bit_cast(bf16x8, a), __builtin_bit_cast(bf16x8, b), c, 0, 0, 0);
}
DI float xmax16(float x) {
  auto r = __builtin_amdgcn_permlane16_swap(__float_as_uint(x), __float_as_uint(x), false, false);
  return fmaxf(__uint_as_float(r[0]), __uint_as_float(r[1]));
}
DI float xmax32(float x) {
  auto r = __builtin_amdgcn_permlane32_swap(__float_as_uint(x), __float_as_uint(x), false, false);
  return fmaxf(__uint_as_float(r[0]), __uint_as_float(r[1]));
}
DI float xsum16(float x) {
  auto r = __builtin_amdgcn_permlane16_swap(__float_as_uint(x), __float_as_uint(x), false, false);
  return __uint_as_float(r[0]) + __uint_as_float(r[1]);
}
DI float xsum32(float x) {
  auto r = __builtin_amdgcn_permlane32_swap(__float_as_uint(x), __float_as_uint(x), false, false);
  return __uint_as_float(r[0]) + __uint_as_float(r[1]);
}
DI float swapadd32(float a, float b) {
  auto r = __builtin_amdgcn_permlane32_swap(__float_as_uint(a), __float_as_uint(b), false, false);
  return __uint_as_float(r[0]) + __uint_as_float(r[1]);
}
DI unsigned umax4(v4u g) { return max(max(g.x, g.y), max(g.z, g.w)); }
DI int wait_flag(rsrc_t ws, unsigned off, int need, unsigned& spins) {
  int v;
  while (true) {
    asm volatile("" ::: "memory");
    v = (int)__builtin_amdgcn_readfirstlane(ld4(ws, off));
    if (v >= need) break;
    if (++spins > SPIN_LIMIT) break;
    if (need < NSEG - LAST_N) __builtin_amdgcn_s_sleep(QA_SLEEP); else __builtin_amdgcn_s_sleep(2);
  }
  return v;
}
constexpr float SCALE = 0.08838834764831845f;
constexpr float LOG2E = 1.4426950408889634f;
constexpr float NEG_MASK = -998244352.0f;

#define TRDECL
#define TRC(i)
#define TRR(i)
#define TRV(i, v)
#define TRFLUSH(s)
constexpr unsigned CH_MEM = 0, CH_QT = 8192, CH_KT = 10240, CH_OL = 12288, CH_KX = 14336, CH_W = 32768;
#define MPAR(a, par) ((((a) >> 13) << 14) | ((par) << 13) | ((a) & 8191u))
typedef unsigned v16u __attribute__((ext_vector_type(16)));
typedef unsigned v32u __attribute__((ext_vector_type(32)));

DI unsigned flag_half(rsrc_t ws, unsigned xph) {
  const unsigned o = OFF_PAR + ((8 - xph) & 7) * 16;
  unsigned f = ld4(ws, o + 12) < ld4(ws, o + 8) ? 8192u : 0u;
  asm("" : "+v"(f));
  return f;
}
DI void chain_role(int h, unsigned parofs, unsigned ldsb, char* wsb, const void* mem0, const void* Wq, const void* Wk, const void* Wv,
                   const void* Wo) {
  const int t = threadIdx.x, lane = t & 63;
  const int w = __builtin_amdgcn_readfirstlane(t >> 6);
  const int gb = lane >> 4, qq = (lane >> 2) & 3, pp = lane & 3;
  constexpr unsigned OOB = 0x40000000u;
  v32u c;
  v16u d;
  c[0] = ldsb + lane * 16;
  c[1] = (lane & 8) ? 0xEEEEu : 0x4444u;
#pragma unroll
  for (int j = 0; j < 4; j++) c[2 + j] = ldsb + CH_MEM + ((j ^ w) * 2048) + lane * 16;
  c[6] = ldsb + (32 * w + 16 * (lane >> 5) + (lane & 15)) * 16 + ((lane >> 4) & 1) * 8;
  c[7] = ldsb + (4 * gb + qq) * 16 + (pp & 1) * 8;
  c[8] = ldsb + ((lane & 8) ? CH_W - 4096 + gb * 256 + ((gb & 1) ^ 1) * 128 : CH_OL + (4 * w + gb) * 128) + (lane & 7) * 16;
  c[9] = ldsb + CH_W + w * 8192 + lane * 16;
  c[10] = c[9] + 65536;
  {
    const int pcol = lane & 15, phf = (lane >> 4) & 1;
    const unsigned pbase = (((8 * w + (lane >> 5)) * 4 + 2 * (pcol >> 3) + phf) * 32 + (h & 7)) * 64 + (pcol & 7) * 8;
    const unsigned ibase = ((h * 4 + w) * 32 + 4 * (lane >> 4) + (lane & 3)) * 64 + ((lane >> 2) & 3) * 16;
    const unsigned mpiece = (2 * h + (w >> 1)) * 128 + (4 * (w & 1) + (lane & 3)) * 16 + (lane >> 4) * 4;
    c[11] = MPAR(pbase, 0u);
    c[12] = MPAR(ibase, 0u);
        c[13] = (lane & 12) == 0 ? mpiece : OOB;
  }
  c[14] = w * 2048 + lane * 16;
  c[15] = w * 512 + lane * 8;
  c[16] = t == 0 ? FLG_M + h * 128 : OOB;
  c[17] = (lane & 8) ? 0u : (lane & 1) ? 0x3f800000u : 0x3f80u;
  c[18] = c[19] = lane < 32 ? 0x3f803f80u : 0u;
  c[20] = c[21] = c[22] = c[23] = 0x3f803f80u;
#pragma unroll
  for (int i = 0; i < 4; i++) {
    unsigned f = (gb & 1) | ((i & 1) << 1) | ((i >> 1) << 2);
    c[24 + i] = gb * 8192 + (((((lane & 15) >> 1) ^ f) << 1) | (lane & 1)) * 16;
    unsigned g = i | (((w >> 1) & 1) << 2);
    c[28 + i] = ((((lane >> 1) ^ g) << 1) | (lane & 1)) * 16;
  }
  {
    const unsigned fq = (qq & 1) | ((gb & 1) << 1) | ((qq >> 1) << 2);
    const unsigned bq = CH_W + (16 * (gb >> 1) + 4 * (gb & 1) + 8 * (qq >> 1) + (qq & 1)) * 256 + pp * 8;
    d[8] = ldsb + bq + (((2 * w) ^ fq) * 32);
    d[9] = ldsb + bq + (((2 * w + 1) ^ fq) * 32);
    const unsigned fo = (qq & 1) | ((qq >> 1) << 1) | ((gb & 1) << 2);
    const unsigned bo = CH_W + (16 * (gb & 1) + 8 * (gb >> 1) + 4 * (qq >> 1) + (qq & 1)) * 1024 + pp * 8;
    d[10] = ldsb + bo + 256 * w;
    d[11] = fo * 32;
    d[0] = d[1] = d[2] = d[3] = d[4] = d[5] = d[6] = d[7] = d[12] = d[13] = 0;
    const unsigned o = w * 2048 + lane * 16;
    const uint64_t ma = (uint64_t)mem0 + ((o >> 4) & 7) * 1024 + (o >> 7) * 16;
    d[14] = (unsigned)ma; d[15] = (unsigned)(ma >> 32);
  }
  const v4u r0 = mksrd(wsb, WS_SPAN), r1 = mksrd(Wq, 512 * 4096 * 2), r2 = mksrd(Wk, 512 * 4096 * 2),
            r3 = mksrd(Wv, 512 * 4096 * 2), r4 = mksrd(Wo, 4096 * 512 * 2);
  const v16u sa = {r0.x, r0.y, r0.z, r0.w, r1.x, r1.y, r1.z, r1.w, r2.x, r2.y, r2.z, r2.w, r3.x, r3.y, r3.z, r3.w};
  const v8u sb = {r4.x, r4.y, r4.z, r4.w, (unsigned)h, (unsigned)w, ldsb, parofs};
  asm volatile(
    "s_mov_b32 s60, 0\n"
    "s_mov_b32 s63, 0\n"
    "buffer_load_dwordx4 v[96:99], off, s[36:39], s59 sc1\n"
    "global_load_dwordx4 v[32:35], v[46:47], off\n"
    "global_load_dwordx4 v[36:39], v[46:47], off offset:128\n"
    "s_mov_b32 s78, 0x3e0293ee\n"
    "s_mov_b32 s79, 0x3e0293ee\n"
    "s_mov_b32 s80, -1\n"
    "s_mov_b32 s81, 0\n"
    "s_lshl_b32 s82, s56, 7\n"
    "s_add_u32 s76, s82, 2146304\n"
    "s_add_u32 s77, s82, 2150400\n"
    "s_lshl_b32 s82, s56, 19\n"
    "s_add_u32 s72, s82, 4194304\n"
    "s_add_u32 s73, s82, 20971520\n"
    "s_lshl_b32 s82, s56, 17\n"
    "s_add_u32 s74, s82, 54525952\n"
    "s_add_u32 s75, s82, 58720256\n"
    "s_lshl_b32 s82, s57, 18\n"
    "s_lshl_b32 s83, s56, 8\n"
    "s_add_u32 s90, s82, s83\n"
    "s_lshl_b32 s82, s57, 13\n"
    "s_add_u32 s91, s82, s58\n"
    "s_add_u32 s91, s91, 32768\n"
    "s_lshl_b32 s82, s56, 17\n"
    "s_lshl_b32 s83, s57, 13\n"
    "s_add_u32 s85, s82, s83\n"
    "s_add_u32 s82, s90, 0\n"
    "s_add_u32 m0, s91, 0\n"
    "s_nop 0\n"
    "buffer_load_dwordx4 v24, s[40:43], s82 offen lds\n"
    "s_add_u32 s82, s90, 32768\n"
    "s_add_u32 m0, s91, 1024\n"
    "s_nop 0\n"
    "buffer_load_dwordx4 v25, s[40:43], s82 offen lds\n"
    "s_add_u32 s82, s90, 65536\n"
    "s_add_u32 m0, s91, 2048\n"
    "s_nop 0\n"
    "buffer_load_dwordx4 v26, s[40:43], s82 offen lds\n"
    "s_add_u32 s82, s90, 98304\n"
    "s_add_u32 m0, s91, 3072\n"
    "s_nop 0\n"
    "buffer_load_dwordx4 v27, s[40:43], s82 offen lds\n"
    "s_add_u32 s82, s90, 131072\n"
    "s_add_u32 m0, s91, 4096\n"
    "s_nop 0\n"
    "buffer_load_dwordx4 v24, s[40:43], s82 offen lds\n"
    "s_add_u32 s82, s90, 163840\n"
    "s_add_u32 m0, s91, 5120\n"
    "s_nop 0\n"
    "buffer_load_dwordx4 v25, s[40:43], s82 offen lds\n"
    "s_add_u32 s82, s90, 196608\n"
    "s_add_u32 m0, s91, 6144\n"
    "s_nop 0\n"
    "buffer_load_dwordx4 v26, s[40:43], s82 offen lds\n"
    "s_add_u32 s82, s90, 229376\n"
    "s_add_u32 m0, s91, 7168\n"
    "s_nop 0\n"
    "buffer_load_dwordx4 v27, s[40:43], s82 offen lds\n"
    "s_add_u32 s82, s90, 1048576\n"
    "s_add_u32 m0, s91, 32768\n"
    "s_nop 0\n"
    "buffer_load_dwordx4 v24, s[40:43], s82 offen lds\n"
    "s_add_u32 s82, s90, 1081344\n"
    "s_add_u32 m0, s91, 33792\n"
    "s_nop 0\n"
    "buffer_load_dwordx4 v25, s[40:43], s82 offen lds\n"
    "s_add_u32 s82, s90, 1114112\n"
    "s_add_u32 m0, s91, 34816\n"
    "s_nop 0\n"
    "buffer_load_dwordx4 v26, s[40:43], s82 offen lds\n"
    "s_add_u32 s82, s90, 1146880\n"
    "s_add_u32 m0, s91, 35840\n"
    "s_nop 0\n"
    "buffer_load_dwordx4 v27, s[40:43], s82 offen lds\n"
    "s_add_u32 s82, s90, 1179648\n"
    "s_add_u32 m0, s91, 36864\n"
    "s_nop 0\n"
    "buffer_load_dwordx4 v24, s[40:43], s82 offen lds\n"
    "s_add_u32 s82, s90, 1212416\n"
    "s_add_u32 m0, s91, 37888\n"
    "s_nop 0\n"
    "buffer_load_dwordx4 v25, s[40:43], s82 offen lds\n"
    "s_add_u32 s82, s90, 1245184\n"
    "s_add_u32 m0, s91, 38912\n"
    "s_nop 0\n"
    "buffer_load_dwordx4 v26, s[40:43], s82 offen lds\n"
    "s_add_u32 s82, s90, 1277952\n"
    "s_add_u32 m0, s91, 39936\n"
    "s_nop 0\n"
    "buffer_load_dwordx4 v27, s[40:43], s82 offen lds\n"
    "s_add_u32 s82, s90, 2097152\n"
    "s_add_u32 m0, s91, 65536\n"
    "s_nop 0\n"
    "buffer_load_dwordx4 v24, s[40:43], s82 offen lds\n"
    "s_add_u32 s82, s90, 2129920\n"
    "s_add_u32 m0, s91, 66560\n"
    "s_nop 0\n"
    "buffer_load_dwordx4 v25, s[40:43], s82 offen lds\n"
    "s_add_u32 s82, s90, 2162688\n"
    "s_add_u32 m0, s91, 67584\n"
    "s_nop 0\n"
    "buffer_load_dwordx4 v26, s[40:43], s82 offen lds\n"
    "s_add_u32 s82, s90, 2195456\n"
    "s_add_u32 m0, s91, 68608\n"
    "s_nop 0\n"
    "buffer_load_dwordx4 v27, s[40:43], s82 offen lds\n"
    "s_add_u32 s82, s90, 2228224\n"
    "s_add_u32 m0, s91, 69632\n"
    "s_nop 0\n"
    "buffer_load_dwordx4 v24, s[40:43], s82 offen lds\n"
    "s_add_u32 s82, s90, 2260992\n"
    "s_add_u32 m0, s91, 70656\n"
    "s_nop 0\n"
    "buffer_load_dwordx4 v25, s[40:43], s82 offen lds\n"
    "s_add_u32 s82, s90, 2293760\n"
    "s_add_u32 m0, s91, 71680\n"
    "s_nop 0\n"
    "buffer_load_dwordx4 v26, s[40:43], s82 offen lds\n"
    "s_add_u32 s82, s90, 2326528\n"
    "s_add_u32 m0, s91, 72704\n"
    "s_nop 0\n"
    "buffer_load_dwordx4 v27, s[40:43], s82 offen lds\n"
    "s_add_u32 s82, s90, 3145728\n"
    "s_add_u32 m0, s91, 98304\n"
    "s_nop 0\n"
    "buffer_load_dwordx4 v24, s[40:43], s82 offen lds\n"
    "s_add_u32 s82, s90, 3178496\n"
    "s_add_u32 m0, s91, 99328\n"
    "s_nop 0\n"
    "buffer_load_dwordx4 v25, s[40:43], s82 offen lds\n"
    "s_add_u32 s82, s90, 3211264\n"
    "s_add_u32 m0, s91, 100352\n"
    "s_nop 0\n"
    "buffer_load_dwordx4 v26, s[40:43], s82 offen lds\n"
    "s_add_u32 s82, s90, 3244032\n"
    "s_add_u32 m0, s91, 101376\n"
    "s_nop 0\n"
    "buffer_load_dwordx4 v27, s[40:43], s82 offen lds\n"
    "s_add_u32 s82, s90, 3276800\n"
    "s_add_u32 m0, s91, 102400\n"
    "s_nop 0\n"
    "buffer_load_dwordx4 v24, s[40:43], s82 offen lds\n"
    "s_add_u32 s82, s90, 3309568\n"
    "s_add_u32 m0, s91, 103424\n"
    "s_nop 0\n"
    "buffer_load_dwordx4 v25, s[40:43], s82 offen lds\n"
    "s_add_u32 s82, s90, 3342336\n"
    "s_add_u32 m0, s91, 104448\n"
    "s_nop 0\n"
    "buffer_load_dwordx4 v26, s[40:43], s82 offen lds\n"
    "s_add_u32 s82, s90, 3375104\n"
    "s_add_u32 m0, s91, 105472\n"
    "s_nop 0\n"
    "buffer_load_dwordx4 v27, s[40:43], s82 offen lds\n"
    "s_lshr_b32 s83, s56, 3\n"
    "s_lshl_b32 s83, s83, 1\n"
    "s_add_u32 s82, s83, 0\n"
    "s_and_b32 s82, s82, 7\n"
    "s_lshl_b32 s82, s82, 5\n"
    "v_xor_b32 v48, s82, v43\n"
    "v_add_u32 v48, v48, v42\n"
    "s_add_u32 s82, s83, 1\n"
    "s_and_b32 s82, s82, 7\n"
    "s_lshl_b32 s82, s82, 5\n"
    "v_xor_b32 v49, s82, v43\n"
    "v_add_u32 v49, v49, v42\n"
    "s_add_u32 s82, s83, 2\n"
    "s_and_b32 s82, s82, 7\n"
    "s_lshl_b32 s82, s82, 5\n"
    "v_xor_b32 v50, s82, v43\n"
    "v_add_u32 v50, v50, v42\n"
    "s_add_u32 s82, s83, 3\n"
    "s_and_b32 s82, s82, 7\n"
    "s_lshl_b32 s82, s82, 5\n"
    "v_xor_b32 v51, s82, v43\n"
    "v_add_u32 v51, v51, v42\n"
    "s_add_u32 s82, s83, 4\n"
    "s_and_b32 s82, s82, 7\n"
    "s_lshl_b32 s82, s82, 5\n"
    "v_xor_b32 v52, s82, v43\n"
    "v_add_u32 v52, v52, v42\n"
    "s_add_u32 s82, s83, 5\n"
    "s_and_b32 s82, s82, 7\n"
    "s_lshl_b32 s82, s82, 5\n"
    "v_xor_b32 v53, s82, v43\n"
    "v_add_u32 v53, v53, v42\n"
    "s_add_u32 s82, s83, 6\n"
    "s_and_b32 s82, s82, 7\n"
    "s_lshl_b32 s82, s82, 5\n"
    "v_xor_b32 v54, s82, v43\n"
    "v_add_u32 v54, v54, v42\n"
    "s_add_u32 s82, s83, 7\n"
    "s_and_b32 s82, s82, 7\n"
    "s_lshl_b32 s82, s82, 5\n"
    "v_xor_b32 v55, s82, v43\n"
    "v_add_u32 v55, v55, v42\n"
    "s_waitcnt vmcnt(24)\n"
    "s_barrier\n"
    "s_cmp_lg_u32 s57, 0\n"
    "s_cbranch_scc1 L_pw0_1_%=\n"
    "ds_read_b64_tr_b16 a[0:1], v40\n"
    "ds_read_b64_tr_b16 a[2:3], v40 offset:512\n"
    "ds_read_b64_tr_b16 a[4:5], v40 offset:8192\n"
    "ds_read_b64_tr_b16 a[6:7], v40 offset:8704\n"
    "ds_read_b64_tr_b16 a[8:9], v41\n"
    "ds_read_b64_tr_b16 a[10:11], v41 offset:512\n"
    "ds_read_b64_tr_b16 a[12:13], v41 offset:8192\n"
    "ds_read_b64_tr_b16 a[14:15], v41 offset:8704\n"
    "ds_read_b64_tr_b16 a[16:17], v40 offset:16384\n"
    "ds_read_b64_tr_b16 a[18:19], v40 offset:16896\n"
    "ds_read_b64_tr_b16 a[20:21], v40 offset:24576\n"
    "ds_read_b64_tr_b16 a[22:23], v40 offset:25088\n"
    "ds_read_b64_tr_b16 a[24:25], v41 offset:16384\n"
    "ds_read_b64_tr_b16 a[26:27], v41 offset:16896\n"
    "ds_read_b64_tr_b16 a[28:29], v41 offset:24576\n"
    "ds_read_b64_tr_b16 a[30:31], v41 offset:25088\n"
    "s_branch L_pe0_%=\n"
    "L_pw0_1_%=:\n"
    "s_cmp_lg_u32 s57, 1\n"
    "s_cbranch_scc1 L_pw0_2_%=\n"
    "ds_read_b64_tr_b16 a[32:33], v40\n"
    "ds_read_b64_tr_b16 a[34:35], v40 offset:512\n"
    "ds_read_b64_tr_b16 a[36:37], v40 offset:8192\n"
    "ds_read_b64_tr_b16 a[38:39], v40 offset:8704\n"
    "ds_read_b64_tr_b16 a[40:41], v41\n"
    "ds_read_b64_tr_b16 a[42:43], v41 offset:512\n"
    "ds_read_b64_tr_b16 a[44:45], v41 offset:8192\n"
    "ds_read_b64_tr_b16 a[46:47], v41 offset:8704\n"
    "ds_read_b64_tr_b16 a[48:49], v40 offset:16384\n"
    "ds_read_b64_tr_b16 a[50:51], v40 offset:16896\n"
    "ds_read_b64_tr_b16 a[52:53], v40 offset:24576\n"
    "ds_read_b64_tr_b16 a[54:55], v40 offset:25088\n"
    "ds_read_b64_tr_b16 a[56:57], v41 offset:16384\n"
    "ds_read_b64_tr_b16 a[58:59], v41 offset:16896\n"
    "ds_read_b64_tr_b16 a[60:61], v41 offset:24576\n"
    "ds_read_b64_tr_b16 a[62:63], v41 offset:25088\n"
    "s_branch L_pe0_%=\n"
    "L_pw0_2_%=:\n"
    "s_cmp_lg_u32 s57, 2\n"
    "s_cbranch_scc1 L_pw0_3_%=\n"
    "ds_read_b64_tr_b16 a[64:65], v40\n"
    "ds_read_b64_tr_b16 a[66:67], v40 offset:512\n"
    "ds_read_b64_tr_b16 a[68:69], v40 offset:8192\n"
    "ds_read_b64_tr_b16 a[70:71], v40 offset:8704\n"
    "ds_read_b64_tr_b16 a[72:73], v41\n"
    "ds_read_b64_tr_b16 a[74:75], v41 offset:512\n"
    "ds_read_b64_tr_b16 a[76:77], v41 offset:8192\n"
    "ds_read_b64_tr_b16 a[78:79], v41 offset:8704\n"
    "ds_read_b64_tr_b16 a[80:81], v40 offset:16384\n"
    "ds_read_b64_tr_b16 a[82:83], v40 offset:16896\n"
    "ds_read_b64_tr_b16 a[84:85], v40 offset:24576\n"
    "ds_read_b64_tr_b16 a[86:87], v40 offset:25088\n"
    "ds_read_b64_tr_b16 a[88:89], v41 offset:16384\n"
    "ds_read_b64_tr_b16 a[90:91], v41 offset:16896\n"
    "ds_read_b64_tr_b16 a[92:93], v41 offset:24576\n"
    "ds_read_b64_tr_b16 a[94:95], v41 offset:25088\n"
    "s_branch L_pe0_%=\n"
    "L_pw0_3_%=:\n"
    "ds_read_b64_tr_b16 a[96:97], v40\n"
    "ds_read_b64_tr_b16 a[98:99], v40 offset:512\n"
    "ds_read_b64_tr_b16 a[100:101], v40 offset:8192\n"
    "ds_read_b64_tr_b16 a[102:103], v40 offset:8704\n"
    "ds_read_b64_tr_b16 a[104:105], v41\n"
    "ds_read_b64_tr_b16 a[106:107], v41 offset:512\n"
    "ds_read_b64_tr_b16 a[108:109], v41 offset:8192\n"
    "ds_read_b64_tr_b16 a[110:111], v41 offset:8704\n"
    "ds_read_b64_tr_b16 a[112:113], v40 offset:16384\n"
    "ds_read_b64_tr_b16 a[114:115], v40 offset:16896\n"
    "ds_read_b64_tr_b16 a[116:117], v40 offset:24576\n"
    "ds_read_b64_tr_b16 a[118:119], v40 offset:25088\n"
    "ds_read_b64_tr_b16 a[120:121], v41 offset:16384\n"
    "ds_read_b64_tr_b16 a[122:123], v41 offset:16896\n"
    "ds_read_b64_tr_b16 a[124:125], v41 offset:24576\n"
    "ds_read_b64_tr_b16 a[126:127], v41 offset:25088\n"
    "L_pe0_%=:\n"
    "s_waitcnt lgkmcnt(0)\n"
    "s_barrier\n"
    "s_add_u32 s82, s90, 0\n"
    "s_add_u32 m0, s91, 0\n"
    "s_nop 0\n"
    "buffer_load_dwordx4 v24, s[44:47], s82 offen lds\n"
    "s_add_u32 s82, s90, 32768\n"
    "s_add_u32 m0, s91, 1024\n"
    "s_nop 0\n"
    "buffer_load_dwordx4 v25, s[44:47], s82 offen lds\n"
    "s_add_u32 s82, s90, 65536\n"
    "s_add_u32 m0, s91, 2048\n"
    "s_nop 0\n"
    "buffer_load_dwordx4 v26, s[44:47], s82 offen lds\n"
    "s_add_u32 s82, s90, 98304\n"
    "s_add_u32 m0, s91, 3072\n"
    "s_nop 0\n"
    "buffer_load_dwordx4 v27, s[44:47], s82 offen lds\n"
    "s_add_u32 s82, s90, 131072\n"
    "s_add_u32 m0, s91, 4096\n"
    "s_nop 0\n"
    "buffer_load_dwordx4 v24, s[44:47], s82 offen lds\n"
    "s_add_u32 s82, s90, 163840\n"
    "s_add_u32 m0, s91, 5120\n"
    "s_nop 0\n"
    "buffer_load_dwordx4 v25, s[44:47], s82 offen lds\n"
    "s_add_u32 s82, s90, 196608\n"
    "s_add_u32 m0, s91, 6144\n"
    "s_nop 0\n"
    "buffer_load_dwordx4 v26, s[44:47], s82 offen lds\n"
    "s_add_u32 s82, s90, 229376\n"
    "s_add_u32 m0, s91, 7168\n"
    "s_nop 0\n"
    "buffer_load_dwordx4 v27, s[44:47], s82 offen lds\n"
    "v_add_u32 v44, 65536, v40\n"
    "v_add_u32 v45, 65536, v41\n"
    "v_add_u32 v56, 65536, v48\n"
    "v_add_u32 v57, 65536, v49\n"
    "v_add_u32 v58, 65536, v50\n"
    "v_add_u32 v59, 65536, v51\n"
    "v_add_u32 v60, 65536, v52\n"
    "v_add_u32 v61, 65536, v53\n"
    "v_add_u32 v62, 65536, v54\n"
    "v_add_u32 v63, 65536, v55\n"
    "s_waitcnt vmcnt(24)\n"
    "s_barrier\n"
    "s_cmp_lg_u32 s57, 0\n"
    "s_cbranch_scc1 L_pw1_1_%=\n"
    "ds_read_b64_tr_b16 a[32:33], v40 offset:32768\n"
    "ds_read_b64_tr_b16 a[34:35], v40 offset:33280\n"
    "ds_read_b64_tr_b16 a[36:37], v40 offset:40960\n"
    "ds_read_b64_tr_b16 a[38:39], v40 offset:41472\n"
    "ds_read_b64_tr_b16 a[40:41], v41 offset:32768\n"
    "ds_read_b64_tr_b16 a[42:43], v41 offset:33280\n"
    "ds_read_b64_tr_b16 a[44:45], v41 offset:40960\n"
    "ds_read_b64_tr_b16 a[46:47], v41 offset:41472\n"
    "ds_read_b64_tr_b16 a[48:49], v40 offset:49152\n"
    "ds_read_b64_tr_b16 a[50:51], v40 offset:49664\n"
    "ds_read_b64_tr_b16 a[52:53], v40 offset:57344\n"
    "ds_read_b64_tr_b16 a[54:55], v40 offset:57856\n"
    "ds_read_b64_tr_b16 a[56:57], v41 offset:49152\n"
    "ds_read_b64_tr_b16 a[58:59], v41 offset:49664\n"
    "ds_read_b64_tr_b16 a[60:61], v41 offset:57344\n"
    "ds_read_b64_tr_b16 a[62:63], v41 offset:57856\n"
    "s_branch L_pe1_%=\n"
    "L_pw1_1_%=:\n"
    "s_cmp_lg_u32 s57, 1\n"
    "s_cbranch_scc1 L_pw1_2_%=\n"
    "ds_read_b64_tr_b16 a[0:1], v40 offset:32768\n"
    "ds_read_b64_tr_b16 a[2:3], v40 offset:33280\n"
    "ds_read_b64_tr_b16 a[4:5], v40 offset:40960\n"
    "ds_read_b64_tr_b16 a[6:7], v40 offset:41472\n"
    "ds_read_b64_tr_b16 a[8:9], v41 offset:32768\n"
    "ds_read_b64_tr_b16 a[10:11], v41 offset:33280\n"
    "ds_read_b64_tr_b16 a[12:13], v41 offset:40960\n"
    "ds_read_b64_tr_b16 a[14:15], v41 offset:41472\n"
    "ds_read_b64_tr_b16 a[16:17], v40 offset:49152\n"
    "ds_read_b64_tr_b16 a[18:19], v40 offset:49664\n"
    "ds_read_b64_tr_b16 a[20:21], v40 offset:57344\n"
    "ds_read_b64_tr_b16 a[22:23], v40 offset:57856\n"
    "ds_read_b64_tr_b16 a[24:25], v41 offset:49152\n"
    "ds_read_b64_tr_b16 a[26:27], v41 offset:49664\n"
    "ds_read_b64_tr_b16 a[28:29], v41 offset:57344\n"
    "ds_read_b64_tr_b16 a[30:31], v41 offset:57856\n"
    "s_branch L_pe1_%=\n"
    "L_pw1_2_%=:\n"
    "s_cmp_lg_u32 s57, 2\n"
    "s_cbranch_scc1 L_pw1_3_%=\n"
    "ds_read_b64_tr_b16 a[96:97], v40 offset:32768\n"
    "ds_read_b64_tr_b16 a[98:99], v40 offset:33280\n"
    "ds_read_b64_tr_b16 a[100:101], v40 offset:40960\n"
    "ds_read_b64_tr_b16 a[102:103], v40 offset:41472\n"
    "ds_read_b64_tr_b16 a[104:105], v41 offset:32768\n"
    "ds_read_b64_tr_b16 a[106:107], v41 offset:33280\n"
    "ds_read_b64_tr_b16 a[108:109], v41 offset:40960\n"
    "ds_read_b64_tr_b16 a[110:111], v41 offset:41472\n"
    "ds_read_b64_tr_b16 a[112:113], v40 offset:49152\n"
    "ds_read_b64_tr_b16 a[114:115], v40 offset:49664\n"
    "ds_read_b64_tr_b16 a[116:117], v40 offset:57344\n"
    "ds_read_b64_tr_b16 a[118:119], v40 offset:57856\n"
    "ds_read_b64_tr_b16 a[120:121], v41 offset:49152\n"
    "ds_read_b64_tr_b16 a[122:123], v41 offset:49664\n"
    "ds_read_b64_tr_b16 a[124:125], v41 offset:57344\n"
    "ds_read_b64_tr_b16 a[126:127], v41 offset:57856\n"
    "s_branch L_pe1_%=\n"
    "L_pw1_3_%=:\n"
    "ds_read_b64_tr_b16 a[64:65], v40 offset:32768\n"
    "ds_read_b64_tr_b16 a[66:67], v40 offset:33280\n"
    "ds_read_b64_tr_b16 a[68:69], v40 offset:40960\n"
    "ds_read_b64_tr_b16 a[70:71], v40 offset:41472\n"
    "ds_read_b64_tr_b16 a[72:73], v41 offset:32768\n"
    "ds_read_b64_tr_b16 a[74:75], v41 offset:33280\n"
    "ds_read_b64_tr_b16 a[76:77], v41 offset:40960\n"
    "ds_read_b64_tr_b16 a[78:79], v41 offset:41472\n"
    "ds_read_b64_tr_b16 a[80:81], v40 offset:49152\n"
    "ds_read_b64_tr_b16 a[82:83], v40 offset:49664\n"
    "ds_read_b64_tr_b16 a[84:85], v40 offset:57344\n"
    "ds_read_b64_tr_b16 a[86:87], v40 offset:57856\n"
    "ds_read_b64_tr_b16 a[88:89], v41 offset:49152\n"
    "ds_read_b64_tr_b16 a[90:91], v41 offset:49664\n"
    "ds_read_b64_tr_b16 a[92:93], v41 offset:57344\n"
    "ds_read_b64_tr_b16 a[94:95], v41 offset:57856\n"
    "L_pe1_%=:\n"
    "s_waitcnt lgkmcnt(0)\n"
    "s_barrier\n"
    "s_add_u32 s82, s90, 1048576\n"
    "s_add_u32 m0, s91, 32768\n"
    "s_nop 0\n"
    "buffer_load_dwordx4 v24, s[44:47], s82 offen lds\n"
    "s_add_u32 s82, s90, 1081344\n"
    "s_add_u32 m0, s91, 33792\n"
    "s_nop 0\n"
    "buffer_load_dwordx4 v25, s[44:47], s82 offen lds\n"
    "s_add_u32 s82, s90, 1114112\n"
    "s_add_u32 m0, s91, 34816\n"
    "s_nop 0\n"
    "buffer_load_dwordx4 v26, s[44:47], s82 offen lds\n"
    "s_add_u32 s82, s90, 1146880\n"
    "s_add_u32 m0, s91, 35840\n"
    "s_nop 0\n"
    "buffer_load_dwordx4 v27, s[44:47], s82 offen lds\n"
    "s_add_u32 s82, s90, 1179648\n"
    "s_add_u32 m0, s91, 36864\n"
    "s_nop 0\n"
    "buffer_load_dwordx4 v24, s[44:47], s82 offen lds\n"
    "s_add_u32 s82, s90, 1212416\n"
    "s_add_u32 m0, s91, 37888\n"
    "s_nop 0\n"
    "buffer_load_dwordx4 v25, s[44:47], s82 offen lds\n"
    "s_add_u32 s82, s90, 1245184\n"
    "s_add_u32 m0, s91, 38912\n"
    "s_nop 0\n"
    "buffer_load_dwordx4 v26, s[44:47], s82 offen lds\n"
    "s_add_u32 s82, s90, 1277952\n"
    "s_add_u32 m0, s91, 39936\n"
    "s_nop 0\n"
    "buffer_load_dwordx4 v27, s[44:47], s82 offen lds\n"
    "s_waitcnt vmcnt(24)\n"
    "s_barrier\n"
    "s_cmp_lg_u32 s57, 0\n"
    "s_cbranch_scc1 L_pw2_1_%=\n"
    "ds_read_b64_tr_b16 a[64:65], v44\n"
    "ds_read_b64_tr_b16 a[66:67], v44 offset:512\n"
    "ds_read_b64_tr_b16 a[68:69], v44 offset:8192\n"
    "ds_read_b64_tr_b16 a[70:71], v44 offset:8704\n"
    "ds_read_b64_tr_b16 a[72:73], v45\n"
    "ds_read_b64_tr_b16 a[74:75], v45 offset:512\n"
    "ds_read_b64_tr_b16 a[76:77], v45 offset:8192\n"
    "ds_read_b64_tr_b16 a[78:79], v45 offset:8704\n"
    "ds_read_b64_tr_b16 a[80:81], v44 offset:16384\n"
    "ds_read_b64_tr_b16 a[82:83], v44 offset:16896\n"
    "ds_read_b64_tr_b16 a[84:85], v44 offset:24576\n"
    "ds_read_b64_tr_b16 a[86:87], v44 offset:25088\n"
    "ds_read_b64_tr_b16 a[88:89], v45 offset:16384\n"
    "ds_read_b64_tr_b16 a[90:91], v45 offset:16896\n"
    "ds_read_b64_tr_b16 a[92:93], v45 offset:24576\n"
    "ds_read_b64_tr_b16 a[94:95], v45 offset:25088\n"
    "s_branch L_pe2_%=\n"
    "L_pw2_1_%=:\n"
    "s_cmp_lg_u32 s57, 1\n"
    "s_cbranch_scc1 L_pw2_2_%=\n"
    "ds_read_b64_tr_b16 a[96:97], v44\n"
    "ds_read_b64_tr_b16 a[98:99], v44 offset:512\n"
    "ds_read_b64_tr_b16 a[100:101], v44 offset:8192\n"
    "ds_read_b64_tr_b16 a[102:103], v44 offset:8704\n"
    "ds_read_b64_tr_b16 a[104:105], v45\n"
    "ds_read_b64_tr_b16 a[106:107], v45 offset:512\n"
    "ds_read_b64_tr_b16 a[108:109], v45 offset:8192\n"
    "ds_read_b64_tr_b16 a[110:111], v45 offset:8704\n"
    "ds_read_b64_tr_b16 a[112:113], v44 offset:16384\n"
    "ds_read_b64_tr_b16 a[114:115], v44 offset:16896\n"
    "ds_read_b64_tr_b16 a[116:117], v44 offset:24576\n"
    "ds_read_b64_tr_b16 a[118:119], v44 offset:25088\n"
    "ds_read_b64_tr_b16 a[120:121], v45 offset:16384\n"
    "ds_read_b64_tr_b16 a[122:123], v45 offset:16896\n"
    "ds_read_b64_tr_b16 a[124:125], v45 offset:24576\n"
    "ds_read_b64_tr_b16 a[126:127], v45 offset:25088\n"
    "s_branch L_pe2_%=\n"
    "L_pw2_2_%=:\n"
    "s_cmp_lg_u32 s57, 2\n"
    "s_cbranch_scc1 L_pw2_3_%=\n"
    "ds_read_b64_tr_b16 a[0:1], v44\n"
    "ds_read_b64_tr_b16 a[2:3], v44 offset:512\n"
    "ds_read_b64_tr_b16 a[4:5], v44 offset:8192\n"
    "ds_read_b64_tr_b16 a[6:7], v44 offset:8704\n"
    "ds_read_b64_tr_b16 a[8:9], v45\n"
    "ds_read_b64_tr_b16 a[10:11], v45 offset:512\n"
    "ds_read_b64_tr_b16 a[12:13], v45 offset:8192\n"
    "ds_read_b64_tr_b16 a[14:15], v45 offset:8704\n"
    "ds_read_b64_tr_b16 a[16:17], v44 offset:16384\n"
    "ds_read_b64_tr_b16 a[18:19], v44 offset:16896\n"
    "ds_read_b64_tr_b16 a[20:21], v44 offset:24576\n"
    "ds_read_b64_tr_b16 a[22:23], v44 offset:25088\n"
    "ds_read_b64_tr_b16 a[24:25], v45 offset:16384\n"
    "ds_read_b64_tr_b16 a[26:27], v45 offset:16896\n"
    "ds_read_b64_tr_b16 a[28:29], v45 offset:24576\n"
    "ds_read_b64_tr_b16 a[30:31], v45 offset:25088\n"
    "s_branch L_pe2_%=\n"
    "L_pw2_3_%=:\n"
    "ds_read_b64_tr_b16 a[32:33], v44\n"
    "ds_read_b64_tr_b16 a[34:35], v44 offset:512\n"
    "ds_read_b64_tr_b16 a[36:37], v44 offset:8192\n"
    "ds_read_b64_tr_b16 a[38:39], v44 offset:8704\n"
    "ds_read_b64_tr_b16 a[40:41], v45\n"
    "ds_read_b64_tr_b16 a[42:43], v45 offset:512\n"
    "ds_read_b64_tr_b16 a[44:45], v45 offset:8192\n"
    "ds_read_b64_tr_b16 a[46:47], v45 offset:8704\n"
    "ds_read_b64_tr_b16 a[48:49], v44 offset:16384\n"
    "ds_read_b64_tr_b16 a[50:51], v44 offset:16896\n"
    "ds_read_b64_tr_b16 a[52:53], v44 offset:24576\n"
    "ds_read_b64_tr_b16 a[54:55], v44 offset:25088\n"
    "ds_read_b64_tr_b16 a[56:57], v45 offset:16384\n"
    "ds_read_b64_tr_b16 a[58:59], v45 offset:16896\n"
    "ds_read_b64_tr_b16 a[60:61], v45 offset:24576\n"
    "ds_read_b64_tr_b16 a[62:63], v45 offset:25088\n"
    "L_pe2_%=:\n"
    "s_waitcnt lgkmcnt(0)\n"
    "s_barrier\n"
    "s_add_u32 s82, s90, 2097152\n"
    "s_add_u32 m0, s91, 65536\n"
    "s_nop 0\n"
    "buffer_load_dwordx4 v24, s[44:47], s82 offen lds\n"
    "s_add_u32 s82, s90, 2129920\n"
    "s_add_u32 m0, s91, 66560\n"
    "s_nop 0\n"
    "buffer_load_dwordx4 v25, s[44:47], s82 offen lds\n"
    "s_add_u32 s82, s90, 2162688\n"
    "s_add_u32 m0, s91, 67584\n"
    "s_nop 0\n"
    "buffer_load_dwordx4 v26, s[44:47], s82 offen lds\n"
    "s_add_u32 s82, s90, 2195456\n"
    "s_add_u32 m0, s91, 68608\n"
    "s_nop 0\n"
    "buffer_load_dwordx4 v27, s[44:47], s82 offen lds\n"
    "s_add_u32 s82, s90, 2228224\n"
    "s_add_u32 m0, s91, 69632\n"
    "s_nop 0\n"
    "buffer_load_dwordx4 v24, s[44:47], s82 offen lds\n"
    "s_add_u32 s82, s90, 2260992\n"
    "s_add_u32 m0, s91, 70656\n"
    "s_nop 0\n"
    "buffer_load_dwordx4 v25, s[44:47], s82 offen lds\n"
    "s_add_u32 s82, s90, 2293760\n"
    "s_add_u32 m0, s91, 71680\n"
    "s_nop 0\n"
    "buffer_load_dwordx4 v26, s[44:47], s82 offen lds\n"
    "s_add_u32 s82, s90, 2326528\n"
    "s_add_u32 m0, s91, 72704\n"
    "s_nop 0\n"
    "buffer_load_dwordx4 v27, s[44:47], s82 offen lds\n"
    "s_waitcnt vmcnt(24)\n"
    "s_barrier\n"
    "s_cmp_lg_u32 s57, 0\n"
    "s_cbranch_scc1 L_pw3_1_%=\n"
    "ds_read_b64_tr_b16 a[96:97], v44 offset:32768\n"
    "ds_read_b64_tr_b16 a[98:99], v44 offset:33280\n"
    "ds_read_b64_tr_b16 a[100:101], v44 offset:40960\n"
    "ds_read_b64_tr_b16 a[102:103], v44 offset:41472\n"
    "ds_read_b64_tr_b16 a[104:105], v45 offset:32768\n"
    "ds_read_b64_tr_b16 a[106:107], v45 offset:33280\n"
    "ds_read_b64_tr_b16 a[108:109], v45 offset:40960\n"
    "ds_read_b64_tr_b16 a[110:111], v45 offset:41472\n"
    "ds_read_b64_tr_b16 a[112:113], v44 offset:49152\n"
    "ds_read_b64_tr_b16 a[114:115], v44 offset:49664\n"
    "ds_read_b64_tr_b16 a[116:117], v44 offset:57344\n"
    "ds_read_b64_tr_b16 a[118:119], v44 offset:57856\n"
    "ds_read_b64_tr_b16 a[120:121], v45 offset:49152\n"
    "ds_read_b64_tr_b16 a[122:123], v45 offset:49664\n"
    "ds_read_b64_tr_b16 a[124:125], v45 offset:57344\n"
    "ds_read_b64_tr_b16 a[126:127], v45 offset:57856\n"
    "s_branch L_pe3_%=\n"
    "L_pw3_1_%=:\n"
    "s_cmp_lg_u32 s57, 1\n"
    "s_cbranch_scc1 L_pw3_2_%=\n"
    "ds_read_b64_tr_b16 a[64:65], v44 offset:32768\n"
    "ds_read_b64_tr_b16 a[66:67], v44 offset:33280\n"
    "ds_read_b64_tr_b16 a[68:69], v44 offset:40960\n"
    "ds_read_b64_tr_b16 a[70:71], v44 offset:41472\n"
    "ds_read_b64_tr_b16 a[72:73], v45 offset:32768\n"
    "ds_read_b64_tr_b16 a[74:75], v45 offset:33280\n"
    "ds_read_b64_tr_b16 a[76:77], v45 offset:40960\n"
    "ds_read_b64_tr_b16 a[78:79], v45 offset:41472\n"
    "ds_read_b64_tr_b16 a[80:81], v44 offset:49152\n"
    "ds_read_b64_tr_b16 a[82:83], v44 offset:49664\n"
    "ds_read_b64_tr_b16 a[84:85], v44 offset:57344\n"
    "ds_read_b64_tr_b16 a[86:87], v44 offset:57856\n"
    "ds_read_b64_tr_b16 a[88:89], v45 offset:49152\n"
    "ds_read_b64_tr_b16 a[90:91], v45 offset:49664\n"
    "ds_read_b64_tr_b16 a[92:93], v45 offset:57344\n"
    "ds_read_b64_tr_b16 a[94:95], v45 offset:57856\n"
    "s_branch L_pe3_%=\n"
    "L_pw3_2_%=:\n"
    "s_cmp_lg_u32 s57, 2\n"
    "s_cbranch_scc1 L_pw3_3_%=\n"
    "ds_read_b64_tr_b16 a[32:33], v44 offset:32768\n"
    "ds_read_b64_tr_b16 a[34:35], v44 offset:33280\n"
    "ds_read_b64_tr_b16 a[36:37], v44 offset:40960\n"
    "ds_read_b64_tr_b16 a[38:39], v44 offset:41472\n"
    "ds_read_b64_tr_b16 a[40:41], v45 offset:32768\n"
    "ds_read_b64_tr_b16 a[42:43], v45 offset:33280\n"
    "ds_read_b64_tr_b16 a[44:45], v45 offset:40960\n"
    "ds_read_b64_tr_b16 a[46:47], v45 offset:41472\n"
    "ds_read_b64_tr_b16 a[48:49], v44 offset:49152\n"
    "ds_read_b64_tr_b16 a[50:51], v44 offset:49664\n"
    "ds_read_b64_tr_b16 a[52:53], v44 offset:57344\n"
    "ds_read_b64_tr_b16 a[54:55], v44 offset:57856\n"
    "ds_read_b64_tr_b16 a[56:57], v45 offset:49152\n"
    "ds_read_b64_tr_b16 a[58:59], v45 offset:49664\n"
    "ds_read_b64_tr_b16 a[60:61], v45 offset:57344\n"
    "ds_read_b64_tr_b16 a[62:63], v45 offset:57856\n"
    "s_branch L_pe3_%=\n"
    "L_pw3_3_%=:\n"
    "ds_read_b64_tr_b16 a[0:1], v44 offset:32768\n"
    "ds_read_b64_tr_b16 a[2:3], v44 offset:33280\n"
    "ds_read_b64_tr_b16 a[4:5], v44 offset:40960\n"
    "ds_read_b64_tr_b16 a[6:7], v44 offset:41472\n"
    "ds_read_b64_tr_b16 a[8:9], v45 offset:32768\n"
    "ds_read_b64_tr_b16 a[10:11], v45 offset:33280\n"
    "ds_read_b64_tr_b16 a[12:13], v45 offset:40960\n"
    "ds_read_b64_tr_b16 a[14:15], v45 offset:41472\n"
    "ds_read_b64_tr_b16 a[16:17], v44 offset:49152\n"
    "ds_read_b64_tr_b16 a[18:19], v44 offset:49664\n"
    "ds_read_b64_tr_b16 a[20:21], v44 offset:57344\n"
    "ds_read_b64_tr_b16 a[22:23], v44 offset:57856\n"
    "ds_read_b64_tr_b16 a[24:25], v45 offset:49152\n"
    "ds_read_b64_tr_b16 a[26:27], v45 offset:49664\n"
    "ds_read_b64_tr_b16 a[28:29], v45 offset:57344\n"
    "ds_read_b64_tr_b16 a[30:31], v45 offset:57856\n"
    "L_pe3_%=:\n"
    "s_waitcnt lgkmcnt(0)\n"
    "s_barrier\n"
    "s_add_u32 s82, s90, 3145728\n"
    "s_add_u32 m0, s91, 98304\n"
    "s_nop 0\n"
    "buffer_load_dwordx4 v24, s[44:47], s82 offen lds\n"
    "s_add_u32 s82, s90, 3178496\n"
    "s_add_u32 m0, s91, 99328\n"
    "s_nop 0\n"
    "buffer_load_dwordx4 v25, s[44:47], s82 offen lds\n"
    "s_add_u32 s82, s90, 3211264\n"
    "s_add_u32 m0, s91, 100352\n"
    "s_nop 0\n"
    "buffer_load_dwordx4 v26, s[44:47], s82 offen lds\n"
    "s_add_u32 s82, s90, 3244032\n"
    "s_add_u32 m0, s91, 101376\n"
    "s_nop 0\n"
    "buffer_load_dwordx4 v27, s[44:47], s82 offen lds\n"
    "s_add_u32 s82, s90, 3276800\n"
    "s_add_u32 m0, s91, 102400\n"
    "s_nop 0\n"
    "buffer_load_dwordx4 v24, s[44:47], s82 offen lds\n"
    "s_add_u32 s82, s90, 3309568\n"
    "s_add_u32 m0, s91, 103424\n"
    "s_nop 0\n"
    "buffer_load_dwordx4 v25, s[44:47], s82 offen lds\n"
    "s_add_u32 s82, s90, 3342336\n"
    "s_add_u32 m0, s91, 104448\n"
    "s_nop 0\n"
    "buffer_load_dwordx4 v26, s[44:47], s82 offen lds\n"
    "s_add_u32 s82, s90, 3375104\n"
    "s_add_u32 m0, s91, 105472\n"
    "s_nop 0\n"
    "buffer_load_dwordx4 v27, s[44:47], s82 offen lds\n"
    "s_waitcnt vmcnt(24)\n"
    "s_barrier\n"
    "s_cmp_lg_u32 s57, 0\n"
    "s_cbranch_scc1 L_pw4_1_%=\n"
    "ds_read_b64_tr_b16 a[128:129], v40\n"
    "ds_read_b64_tr_b16 a[130:131], v40 offset:512\n"
    "ds_read_b64_tr_b16 a[132:133], v40 offset:8192\n"
    "ds_read_b64_tr_b16 a[134:135], v40 offset:8704\n"
    "ds_read_b64_tr_b16 a[136:137], v41\n"
    "ds_read_b64_tr_b16 a[138:139], v41 offset:512\n"
    "ds_read_b64_tr_b16 a[140:141], v41 offset:8192\n"
    "ds_read_b64_tr_b16 a[142:143], v41 offset:8704\n"
    "ds_read_b64_tr_b16 a[144:145], v40 offset:16384\n"
    "ds_read_b64_tr_b16 a[146:147], v40 offset:16896\n"
    "ds_read_b64_tr_b16 a[148:149], v40 offset:24576\n"
    "ds_read_b64_tr_b16 a[150:151], v40 offset:25088\n"
    "ds_read_b64_tr_b16 a[152:153], v41 offset:16384\n"
    "ds_read_b64_tr_b16 a[154:155], v41 offset:16896\n"
    "ds_read_b64_tr_b16 a[156:157], v41 offset:24576\n"
    "ds_read_b64_tr_b16 a[158:159], v41 offset:25088\n"
    "s_branch L_pe4_%=\n"
    "L_pw4_1_%=:\n"
    "s_cmp_lg_u32 s57, 1\n"
    "s_cbranch_scc1 L_pw4_2_%=\n"
    "ds_read_b64_tr_b16 a[160:161], v40\n"
    "ds_read_b64_tr_b16 a[162:163], v40 offset:512\n"
    "ds_read_b64_tr_b16 a[164:165], v40 offset:8192\n"
    "ds_read_b64_tr_b16 a[166:167], v40 offset:8704\n"
    "ds_read_b64_tr_b16 a[168:169], v41\n"
    "ds_read_b64_tr_b16 a[170:171], v41 offset:512\n"
    "ds_read_b64_tr_b16 a[172:173], v41 offset:8192\n"
    "ds_read_b64_tr_b16 a[174:175], v41 offset:8704\n"
    "ds_read_b64_tr_b16 a[176:177], v40 offset:16384\n"
    "ds_read_b64_tr_b16 a[178:179], v40 offset:16896\n"
    "ds_read_b64_tr_b16 a[180:181], v40 offset:24576\n"
    "ds_read_b64_tr_b16 a[182:183], v40 offset:25088\n"
    "ds_read_b64_tr_b16 a[184:185], v41 offset:16384\n"
    "ds_read_b64_tr_b16 a[186:187], v41 offset:16896\n"
    "ds_read_b64_tr_b16 a[188:189], v41 offset:24576\n"
    "ds_read_b64_tr_b16 a[190:191], v41 offset:25088\n"
    "s_branch L_pe4_%=\n"
    "L_pw4_2_%=:\n"
    "s_cmp_lg_u32 s57, 2\n"
    "s_cbranch_scc1 L_pw4_3_%=\n"
    "ds_read_b64_tr_b16 a[192:193], v40\n"
    "ds_read_b64_tr_b16 a[194:195], v40 offset:512\n"
    "ds_read_b64_tr_b16 a[196:197], v40 offset:8192\n"
    "ds_read_b64_tr_b16 a[198:199], v40 offset:8704\n"
    "ds_read_b64_tr_b16 a[200:201], v41\n"
    "ds_read_b64_tr_b16 a[202:203], v41 offset:512\n"
    "ds_read_b64_tr_b16 a[204:205], v41 offset:8192\n"
    "ds_read_b64_tr_b16 a[206:207], v41 offset:8704\n"
    "ds_read_b64_tr_b16 a[208:209], v40 offset:16384\n"
    "ds_read_b64_tr_b16 a[210:211], v40 offset:16896\n"
    "ds_read_b64_tr_b16 a[212:213], v40 offset:24576\n"
    "ds_read_b64_tr_b16 a[214:215], v40 offset:25088\n"
    "ds_read_b64_tr_b16 a[216:217], v41 offset:16384\n"
    "ds_read_b64_tr_b16 a[218:219], v41 offset:16896\n"
    "ds_read_b64_tr_b16 a[220:221], v41 offset:24576\n"
    "ds_read_b64_tr_b16 a[222:223], v41 offset:25088\n"
    "s_branch L_pe4_%=\n"
    "L_pw4_3_%=:\n"
    "ds_read_b64_tr_b16 a[224:225], v40\n"
    "ds_read_b64_tr_b16 a[226:227], v40 offset:512\n"
    "ds_read_b64_tr_b16 a[228:229], v40 offset:8192\n"
    "ds_read_b64_tr_b16 a[230:231], v40 offset:8704\n"
    "ds_read_b64_tr_b16 a[232:233], v41\n"
    "ds_read_b64_tr_b16 a[234:235], v41 offset:512\n"
    "ds_read_b64_tr_b16 a[236:237], v41 offset:8192\n"
    "ds_read_b64_tr_b16 a[238:239], v41 offset:8704\n"
    "ds_read_b64_tr_b16 a[240:241], v40 offset:16384\n"
    "ds_read_b64_tr_b16 a[242:243], v40 offset:16896\n"
    "ds_read_b64_tr_b16 a[244:245], v40 offset:24576\n"
    "ds_read_b64_tr_b16 a[246:247], v40 offset:25088\n"
    "ds_read_b64_tr_b16 a[248:249], v41 offset:16384\n"
    "ds_read_b64_tr_b16 a[250:251], v41 offset:16896\n"
    "ds_read_b64_tr_b16 a[252:253], v41 offset:24576\n"
    "ds_read_b64_tr_b16 a[254:255], v41 offset:25088\n"
    "L_pe4_%=:\n"
    "s_waitcnt lgkmcnt(0)\n"
    "s_barrier\n"
    "s_add_u32 s82, s90, 0\n"
    "s_add_u32 m0, s91, 0\n"
    "s_nop 0\n"
    "buffer_load_dwordx4 v24, s[48:51], s82 offen lds\n"
    "s_add_u32 s82, s90, 32768\n"
    "s_add_u32 m0, s91, 1024\n"
    "s_nop 0\n"
    "buffer_load_dwordx4 v25, s[48:51], s82 offen lds\n"
    "s_add_u32 s82, s90, 65536\n"
    "s_add_u32 m0, s91, 2048\n"
    "s_nop 0\n"
    "buffer_load_dwordx4 v26, s[48:51], s82 offen lds\n"
    "s_add_u32 s82, s90, 98304\n"
    "s_add_u32 m0, s91, 3072\n"
    "s_nop 0\n"
    "buffer_load_dwordx4 v27, s[48:51], s82 offen lds\n"
    "s_add_u32 s82, s90, 131072\n"
    "s_add_u32 m0, s91, 4096\n"
    "s_nop 0\n"
    "buffer_load_dwordx4 v24, s[48:51], s82 offen lds\n"
    "s_add_u32 s82, s90, 163840\n"
    "s_add_u32 m0, s91, 5120\n"
    "s_nop 0\n"
    "buffer_load_dwordx4 v25, s[48:51], s82 offen lds\n"
    "s_add_u32 s82, s90, 196608\n"
    "s_add_u32 m0, s91, 6144\n"
    "s_nop 0\n"
    "buffer_load_dwordx4 v26, s[48:51], s82 offen lds\n"
    "s_add_u32 s82, s90, 229376\n"
    "s_add_u32 m0, s91, 7168\n"
    "s_nop 0\n"
    "buffer_load_dwordx4 v27, s[48:51], s82 offen lds\n"
    "s_waitcnt vmcnt(24)\n"
    "s_barrier\n"
    "s_cmp_lg_u32 s57, 0\n"
    "s_cbranch_scc1 L_pw5_1_%=\n"
    "ds_read_b64_tr_b16 a[160:161], v40 offset:32768\n"
    "ds_read_b64_tr_b16 a[162:163], v40 offset:33280\n"
    "ds_read_b64_tr_b16 a[164:165], v40 offset:40960\n"
    "ds_read_b64_tr_b16 a[166:167], v40 offset:41472\n"
    "ds_read_b64_tr_b16 a[168:169], v41 offset:32768\n"
    "ds_read_b64_tr_b16 a[170:171], v41 offset:33280\n"
    "ds_read_b64_tr_b16 a[172:173], v41 offset:40960\n"
    "ds_read_b64_tr_b16 a[174:175], v41 offset:41472\n"
    "ds_read_b64_tr_b16 a[176:177], v40 offset:49152\n"
    "ds_read_b64_tr_b16 a[178:179], v40 offset:49664\n"
    "ds_read_b64_tr_b16 a[180:181], v40 offset:57344\n"
    "ds_read_b64_tr_b16 a[182:183], v40 offset:57856\n"
    "ds_read_b64_tr_b16 a[184:185], v41 offset:49152\n"
    "ds_read_b64_tr_b16 a[186:187], v41 offset:49664\n"
    "ds_read_b64_tr_b16 a[188:189], v41 offset:57344\n"
    "ds_read_b64_tr_b16 a[190:191], v41 offset:57856\n"
    "s_branch L_pe5_%=\n"
    "L_pw5_1_%=:\n"
    "s_cmp_lg_u32 s57, 1\n"
    "s_cbranch_scc1 L_pw5_2_%=\n"
    "ds_read_b64_tr_b16 a[128:129], v40 offset:32768\n"
    "ds_read_b64_tr_b16 a[130:131], v40 offset:33280\n"
    "ds_read_b64_tr_b16 a[132:133], v40 offset:40960\n"
    "ds_read_b64_tr_b16 a[134:135], v40 offset:41472\n"
    "ds_read_b64_tr_b16 a[136:137], v41 offset:32768\n"
    "ds_read_b64_tr_b16 a[138:139], v41 offset:33280\n"
    "ds_read_b64_tr_b16 a[140:141], v41 offset:40960\n"
    "ds_read_b64_tr_b16 a[142:143], v41 offset:41472\n"
    "ds_read_b64_tr_b16 a[144:145], v40 offset:49152\n"
    "ds_read_b64_tr_b16 a[146:147], v40 offset:49664\n"
    "ds_read_b64_tr_b16 a[148:149], v40 offset:57344\n"
    "ds_read_b64_tr_b16 a[150:151], v40 offset:57856\n"
    "ds_read_b64_tr_b16 a[152:153], v41 offset:49152\n"
    "ds_read_b64_tr_b16 a[154:155], v41 offset:49664\n"
    "ds_read_b64_tr_b16 a[156:157], v41 offset:57344\n"
    "ds_read_b64_tr_b16 a[158:159], v41 offset:57856\n"
    "s_branch L_pe5_%=\n"
    "L_pw5_2_%=:\n"
    "s_cmp_lg_u32 s57, 2\n"
    "s_cbranch_scc1 L_pw5_3_%=\n"
    "ds_read_b64_tr_b16 a[224:225], v40 offset:32768\n"
    "ds_read_b64_tr_b16 a[226:227], v40 offset:33280\n"
    "ds_read_b64_tr_b16 a[228:229], v40 offset:40960\n"
    "ds_read_b64_tr_b16 a[230:231], v40 offset:41472\n"
    "ds_read_b64_tr_b16 a[232:233], v41 offset:32768\n"
    "ds_read_b64_tr_b16 a[234:235], v41 offset:33280\n"
    "ds_read_b64_tr_b16 a[236:237], v41 offset:40960\n"
    "ds_read_b64_tr_b16 a[238:239], v41 offset:41472\n"
    "ds_read_b64_tr_b16 a[240:241], v40 offset:49152\n"
    "ds_read_b64_tr_b16 a[242:243], v40 offset:49664\n"
    "ds_read_b64_tr_b16 a[244:245], v40 offset:57344\n"
    "ds_read_b64_tr_b16 a[246:247], v40 offset:57856\n"
    "ds_read_b64_tr_b16 a[248:249], v41 offset:49152\n"
    "ds_read_b64_tr_b16 a[250:251], v41 offset:49664\n"
    "ds_read_b64_tr_b16 a[252:253], v41 offset:57344\n"
    "ds_read_b64_tr_b16 a[254:255], v41 offset:57856\n"
    "s_branch L_pe5_%=\n"
    "L_pw5_3_%=:\n"
    "ds_read_b64_tr_b16 a[192:193], v40 offset:32768\n"
    "ds_read_b64_tr_b16 a[194:195], v40 offset:33280\n"
    "ds_read_b64_tr_b16 a[196:197], v40 offset:40960\n"
    "ds_read_b64_tr_b16 a[198:199], v40 offset:41472\n"
    "ds_read_b64_tr_b16 a[200:201], v41 offset:32768\n"
    "ds_read_b64_tr_b16 a[202:203], v41 offset:33280\n"
    "ds_read_b64_tr_b16 a[204:205], v41 offset:40960\n"
    "ds_read_b64_tr_b16 a[206:207], v41 offset:41472\n"
    "ds_read_b64_tr_b16 a[208:209], v40 offset:49152\n"
    "ds_read_b64_tr_b16 a[210:211], v40 offset:49664\n"
    "ds_read_b64_tr_b16 a[212:213], v40 offset:57344\n"
    "ds_read_b64_tr_b16 a[214:215], v40 offset:57856\n"
    "ds_read_b64_tr_b16 a[216:217], v41 offset:49152\n"
    "ds_read_b64_tr_b16 a[218:219], v41 offset:49664\n"
    "ds_read_b64_tr_b16 a[220:221], v41 offset:57344\n"
    "ds_read_b64_tr_b16 a[222:223], v41 offset:57856\n"
    "L_pe5_%=:\n"
    "s_waitcnt lgkmcnt(0)\n"
    "s_barrier\n"
    "s_add_u32 s82, s90, 1048576\n"
    "s_add_u32 m0, s91, 32768\n"
    "s_nop 0\n"
    "buffer_load_dwordx4 v24, s[48:51], s82 offen lds\n"
    "s_add_u32 s82, s90, 1081344\n"
    "s_add_u32 m0, s91, 33792\n"
    "s_nop 0\n"
    "buffer_load_dwordx4 v25, s[48:51], s82 offen lds\n"
    "s_add_u32 s82, s90, 1114112\n"
    "s_add_u32 m0, s91, 34816\n"
    "s_nop 0\n"
    "buffer_load_dwordx4 v26, s[48:51], s82 offen lds\n"
    "s_add_u32 s82, s90, 1146880\n"
    "s_add_u32 m0, s91, 35840\n"
    "s_nop 0\n"
    "buffer_load_dwordx4 v27, s[48:51], s82 offen lds\n"
    "s_add_u32 s82, s90, 1179648\n"
    "s_add_u32 m0, s91, 36864\n"
    "s_nop 0\n"
    "buffer_load_dwordx4 v24, s[48:51], s82 offen lds\n"
    "s_add_u32 s82, s90, 1212416\n"
    "s_add_u32 m0, s91, 37888\n"
    "s_nop 0\n"
    "buffer_load_dwordx4 v25, s[48:51], s82 offen lds\n"
    "s_add_u32 s82, s90, 1245184\n"
    "s_add_u32 m0, s91, 38912\n"
    "s_nop 0\n"
    "buffer_load_dwordx4 v26, s[48:51], s82 offen lds\n"
    "s_add_u32 s82, s90, 1277952\n"
    "s_add_u32 m0, s91, 39936\n"
    "s_nop 0\n"
    "buffer_load_dwordx4 v27, s[48:51], s82 offen lds\n"
    "s_waitcnt vmcnt(24)\n"
    "s_barrier\n"
    "s_cmp_lg_u32 s57, 0\n"
    "s_cbranch_scc1 L_pw6_1_%=\n"
    "ds_read_b64_tr_b16 a[192:193], v44\n"
    "ds_read_b64_tr_b16 a[194:195], v44 offset:512\n"
    "ds_read_b64_tr_b16 a[196:197], v44 offset:8192\n"
    "ds_read_b64_tr_b16 a[198:199], v44 offset:8704\n"
    "ds_read_b64_tr_b16 a[200:201], v45\n"
    "ds_read_b64_tr_b16 a[202:203], v45 offset:512\n"
    "ds_read_b64_tr_b16 a[204:205], v45 offset:8192\n"
    "ds_read_b64_tr_b16 a[206:207], v45 offset:8704\n"
    "ds_read_b64_tr_b16 a[208:209], v44 offset:16384\n"
    "ds_read_b64_tr_b16 a[210:211], v44 offset:16896\n"
    "ds_read_b64_tr_b16 a[212:213], v44 offset:24576\n"
    "ds_read_b64_tr_b16 a[214:215], v44 offset:25088\n"
    "ds_read_b64_tr_b16 a[216:217], v45 offset:16384\n"
    "ds_read_b64_tr_b16 a[218:219], v45 offset:16896\n"
    "ds_read_b64_tr_b16 a[220:221], v45 offset:24576\n"
    "ds_read_b64_tr_b16 a[222:223], v45 offset:25088\n"
    "s_branch L_pe6_%=\n"
    "L_pw6_1_%=:\n"
    "s_cmp_lg_u32 s57, 1\n"
    "s_cbranch_scc1 L_pw6_2_%=\n"
    "ds_read_b64_tr_b16 a[224:225], v44\n"
    "ds_read_b64_tr_b16 a[226:227], v44 offset:512\n"
    "ds_read_b64_tr_b16 a[228:229], v44 offset:8192\n"
    "ds_read_b64_tr_b16 a[230:231], v44 offset:8704\n"
    "ds_read_b64_tr_b16 a[232:233], v45\n"
    "ds_read_b64_tr_b16 a[234:235], v45 offset:512\n"
    "ds_read_b64_tr_b16 a[236:237], v45 offset:8192\n"
    "ds_read_b64_tr_b16 a[238:239], v45 offset:8704\n"
    "ds_read_b64_tr_b16 a[240:241], v44 offset:16384\n"
    "ds_read_b64_tr_b16 a[242:243], v44 offset:16896\n"
    "ds_read_b64_tr_b16 a[244:245], v44 offset:24576\n"
    "ds_read_b64_tr_b16 a[246:247], v44 offset:25088\n"
    "ds_read_b64_tr_b16 a[248:249], v45 offset:16384\n"
    "ds_read_b64_tr_b16 a[250:251], v45 offset:16896\n"
    "ds_read_b64_tr_b16 a[252:253], v45 offset:24576\n"
    "ds_read_b64_tr_b16 a[254:255], v45 offset:25088\n"
    "s_branch L_pe6_%=\n"
    "L_pw6_2_%=:\n"
    "s_cmp_lg_u32 s57, 2\n"
    "s_cbranch_scc1 L_pw6_3_%=\n"
    "ds_read_b64_tr_b16 a[128:129], v44\n"
    "ds_read_b64_tr_b16 a[130:131], v44 offset:512\n"
    "ds_read_b64_tr_b16 a[132:133], v44 offset:8192\n"
    "ds_read_b64_tr_b16 a[134:135], v44 offset:8704\n"
    "ds_read_b64_tr_b16 a[136:137], v45\n"
    "ds_read_b64_tr_b16 a[138:139], v45 offset:512\n"
    "ds_read_b64_tr_b16 a[140:141], v45 offset:8192\n"
    "ds_read_b64_tr_b16 a[142:143], v45 offset:8704\n"
    "ds_read_b64_tr_b16 a[144:145], v44 offset:16384\n"
    "ds_read_b64_tr_b16 a[146:147], v44 offset:16896\n"
    "ds_read_b64_tr_b16 a[148:149], v44 offset:24576\n"
    "ds_read_b64_tr_b16 a[150:151], v44 offset:25088\n"
    "ds_read_b64_tr_b16 a[152:153], v45 offset:16384\n"
    "ds_read_b64_tr_b16 a[154:155], v45 offset:16896\n"
    "ds_read_b64_tr_b16 a[156:157], v45 offset:24576\n"
    "ds_read_b64_tr_b16 a[158:159], v45 offset:25088\n"
    "s_branch L_pe6_%=\n"
    "L_pw6_3_%=:\n"
    "ds_read_b64_tr_b16 a[160:161], v44\n"
    "ds_read_b64_tr_b16 a[162:163], v44 offset:512\n"
    "ds_read_b64_tr_b16 a[164:165], v44 offset:8192\n"
    "ds_read_b64_tr_b16 a[166:167], v44 offset:8704\n"
    "ds_read_b64_tr_b16 a[168:169], v45\n"
    "ds_read_b64_tr_b16 a[170:171], v45 offset:512\n"
    "ds_read_b64_tr_b16 a[172:173], v45 offset:8192\n"
    "ds_read_b64_tr_b16 a[174:175], v45 offset:8704\n"
    "ds_read_b64_tr_b16 a[176:177], v44 offset:16384\n"
    "ds_read_b64_tr_b16 a[178:179], v44 offset:16896\n"
    "ds_read_b64_tr_b16 a[180:181], v44 offset:24576\n"
    "ds_read_b64_tr_b16 a[182:183], v44 offset:25088\n"
    "ds_read_b64_tr_b16 a[184:185], v45 offset:16384\n"
    "ds_read_b64_tr_b16 a[186:187], v45 offset:16896\n"
    "ds_read_b64_tr_b16 a[188:189], v45 offset:24576\n"
    "ds_read_b64_tr_b16 a[190:191], v45 offset:25088\n"
    "L_pe6_%=:\n"
    "s_waitcnt lgkmcnt(0)\n"
    "s_barrier\n"
    "s_add_u32 s82, s90, 2097152\n"
    "s_add_u32 m0, s91, 65536\n"
    "s_nop 0\n"
    "buffer_load_dwordx4 v24, s[48:51], s82 offen lds\n"
    "s_add_u32 s82, s90, 2129920\n"
    "s_add_u32 m0, s91, 66560\n"
    "s_nop 0\n"
    "buffer_load_dwordx4 v25, s[48:51], s82 offen lds\n"
    "s_add_u32 s82, s90, 2162688\n"
    "s_add_u32 m0, s91, 67584\n"
    "s_nop 0\n"
    "buffer_load_dwordx4 v26, s[48:51], s82 offen lds\n"
    "s_add_u32 s82, s90, 2195456\n"
    "s_add_u32 m0, s91, 68608\n"
    "s_nop 0\n"
    "buffer_load_dwordx4 v27, s[48:51], s82 offen lds\n"
    "s_add_u32 s82, s90, 2228224\n"
    "s_add_u32 m0, s91, 69632\n"
    "s_nop 0\n"
    "buffer_load_dwordx4 v24, s[48:51], s82 offen lds\n"
    "s_add_u32 s82, s90, 2260992\n"
    "s_add_u32 m0, s91, 70656\n"
    "s_nop 0\n"
    "buffer_load_dwordx4 v25, s[48:51], s82 offen lds\n"
    "s_add_u32 s82, s90, 2293760\n"
    "s_add_u32 m0, s91, 71680\n"
    "s_nop 0\n"
    "buffer_load_dwordx4 v26, s[48:51], s82 offen lds\n"
    "s_add_u32 s82, s90, 2326528\n"
    "s_add_u32 m0, s91, 72704\n"
    "s_nop 0\n"
    "buffer_load_dwordx4 v27, s[48:51], s82 offen lds\n"
    "s_waitcnt vmcnt(24)\n"
    "s_barrier\n"
    "s_cmp_lg_u32 s57, 0\n"
    "s_cbranch_scc1 L_pw7_1_%=\n"
    "ds_read_b64_tr_b16 a[224:225], v44 offset:32768\n"
    "ds_read_b64_tr_b16 a[226:227], v44 offset:33280\n"
    "ds_read_b64_tr_b16 a[228:229], v44 offset:40960\n"
    "ds_read_b64_tr_b16 a[230:231], v44 offset:41472\n"
    "ds_read_b64_tr_b16 a[232:233], v45 offset:32768\n"
    "ds_read_b64_tr_b16 a[234:235], v45 offset:33280\n"
    "ds_read_b64_tr_b16 a[236:237], v45 offset:40960\n"
    "ds_read_b64_tr_b16 a[238:239], v45 offset:41472\n"
    "ds_read_b64_tr_b16 a[240:241], v44 offset:49152\n"
    "ds_read_b64_tr_b16 a[242:243], v44 offset:49664\n"
    "ds_read_b64_tr_b16 a[244:245], v44 offset:57344\n"
    "ds_read_b64_tr_b16 a[246:247], v44 offset:57856\n"
    "ds_read_b64_tr_b16 a[248:249], v45 offset:49152\n"
    "ds_read_b64_tr_b16 a[250:251], v45 offset:49664\n"
    "ds_read_b64_tr_b16 a[252:253], v45 offset:57344\n"
    "ds_read_b64_tr_b16 a[254:255], v45 offset:57856\n"
    "s_branch L_pe7_%=\n"
    "L_pw7_1_%=:\n"
    "s_cmp_lg_u32 s57, 1\n"
    "s_cbranch_scc1 L_pw7_2_%=\n"
    "ds_read_b64_tr_b16 a[192:193], v44 offset:32768\n"
    "ds_read_b64_tr_b16 a[194:195], v44 offset:33280\n"
    "ds_read_b64_tr_b16 a[196:197], v44 offset:40960\n"
    "ds_read_b64_tr_b16 a[198:199], v44 offset:41472\n"
    "ds_read_b64_tr_b16 a[200:201], v45 offset:32768\n"
    "ds_read_b64_tr_b16 a[202:203], v45 offset:33280\n"
    "ds_read_b64_tr_b16 a[204:205], v45 offset:40960\n"
    "ds_read_b64_tr_b16 a[206:207], v45 offset:41472\n"
    "ds_read_b64_tr_b16 a[208:209], v44 offset:49152\n"
    "ds_read_b64_tr_b16 a[210:211], v44 offset:49664\n"
    "ds_read_b64_tr_b16 a[212:213], v44 offset:57344\n"
    "ds_read_b64_tr_b16 a[214:215], v44 offset:57856\n"
    "ds_read_b64_tr_b16 a[216:217], v45 offset:49152\n"
    "ds_read_b64_tr_b16 a[218:219], v45 offset:49664\n"
    "ds_read_b64_tr_b16 a[220:221], v45 offset:57344\n"
    "ds_read_b64_tr_b16 a[222:223], v45 offset:57856\n"
    "s_branch L_pe7_%=\n"
    "L_pw7_2_%=:\n"
    "s_cmp_lg_u32 s57, 2\n"
    "s_cbranch_scc1 L_pw7_3_%=\n"
    "ds_read_b64_tr_b16 a[160:161], v44 offset:32768\n"
    "ds_read_b64_tr_b16 a[162:163], v44 offset:33280\n"
    "ds_read_b64_tr_b16 a[164:165], v44 offset:40960\n"
    "ds_read_b64_tr_b16 a[166:167], v44 offset:41472\n"
    "ds_read_b64_tr_b16 a[168:169], v45 offset:32768\n"
    "ds_read_b64_tr_b16 a[170:171], v45 offset:33280\n"
    "ds_read_b64_tr_b16 a[172:173], v45 offset:40960\n"
    "ds_read_b64_tr_b16 a[174:175], v45 offset:41472\n"
    "ds_read_b64_tr_b16 a[176:177], v44 offset:49152\n"
    "ds_read_b64_tr_b16 a[178:179], v44 offset:49664\n"
    "ds_read_b64_tr_b16 a[180:181], v44 offset:57344\n"
    "ds_read_b64_tr_b16 a[182:183], v44 offset:57856\n"
    "ds_read_b64_tr_b16 a[184:185], v45 offset:49152\n"
    "ds_read_b64_tr_b16 a[186:187], v45 offset:49664\n"
    "ds_read_b64_tr_b16 a[188:189], v45 offset:57344\n"
    "ds_read_b64_tr_b16 a[190:191], v45 offset:57856\n"
    "s_branch L_pe7_%=\n"
    "L_pw7_3_%=:\n"
    "ds_read_b64_tr_b16 a[128:129], v44 offset:32768\n"
    "ds_read_b64_tr_b16 a[130:131], v44 offset:33280\n"
    "ds_read_b64_tr_b16 a[132:133], v44 offset:40960\n"
    "ds_read_b64_tr_b16 a[134:135], v44 offset:41472\n"
    "ds_read_b64_tr_b16 a[136:137], v45 offset:32768\n"
    "ds_read_b64_tr_b16 a[138:139], v45 offset:33280\n"
    "ds_read_b64_tr_b16 a[140:141], v45 offset:40960\n"
    "ds_read_b64_tr_b16 a[142:143], v45 offset:41472\n"
    "ds_read_b64_tr_b16 a[144:145], v44 offset:49152\n"
    "ds_read_b64_tr_b16 a[146:147], v44 offset:49664\n"
    "ds_read_b64_tr_b16 a[148:149], v44 offset:57344\n"
    "ds_read_b64_tr_b16 a[150:151], v44 offset:57856\n"
    "ds_read_b64_tr_b16 a[152:153], v45 offset:49152\n"
    "ds_read_b64_tr_b16 a[154:155], v45 offset:49664\n"
    "ds_read_b64_tr_b16 a[156:157], v45 offset:57344\n"
    "ds_read_b64_tr_b16 a[158:159], v45 offset:57856\n"
    "L_pe7_%=:\n"
    "s_waitcnt lgkmcnt(0)\n"
    "s_barrier\n"
    "s_add_u32 s82, s90, 3145728\n"
    "s_add_u32 m0, s91, 98304\n"
    "s_nop 0\n"
    "buffer_load_dwordx4 v24, s[48:51], s82 offen lds\n"
    "s_add_u32 s82, s90, 3178496\n"
    "s_add_u32 m0, s91, 99328\n"
    "s_nop 0\n"
    "buffer_load_dwordx4 v25, s[48:51], s82 offen lds\n"
    "s_add_u32 s82, s90, 3211264\n"
    "s_add_u32 m0, s91, 100352\n"
    "s_nop 0\n"
    "buffer_load_dwordx4 v26, s[48:51], s82 offen lds\n"
    "s_add_u32 s82, s90, 3244032\n"
    "s_add_u32 m0, s91, 101376\n"
    "s_nop 0\n"
    "buffer_load_dwordx4 v27, s[48:51], s82 offen lds\n"
    "s_add_u32 s82, s90, 3276800\n"
    "s_add_u32 m0, s91, 102400\n"
    "s_nop 0\n"
    "buffer_load_dwordx4 v24, s[48:51], s82 offen lds\n"
    "s_add_u32 s82, s90, 3309568\n"
    "s_add_u32 m0, s91, 103424\n"
    "s_nop 0\n"
    "buffer_load_dwordx4 v25, s[48:51], s82 offen lds\n"
    "s_add_u32 s82, s90, 3342336\n"
    "s_add_u32 m0, s91, 104448\n"
    "s_nop 0\n"
    "buffer_load_dwordx4 v26, s[48:51], s82 offen lds\n"
    "s_add_u32 s82, s90, 3375104\n"
    "s_add_u32 m0, s91, 105472\n"
    "s_nop 0\n"
    "buffer_load_dwordx4 v27, s[48:51], s82 offen lds\n"
    "s_waitcnt vmcnt(24)\n"
    "s_barrier\n"
    "s_cmp_lg_u32 s57, 0\n"
    "s_cbranch_scc1 L_pw8_1_%=\n"
    "ds_read_b64_tr_b16 v[128:129], v40\n"
    "ds_read_b64_tr_b16 v[130:131], v40 offset:512\n"
    "ds_read_b64_tr_b16 v[132:133], v40 offset:8192\n"
    "ds_read_b64_tr_b16 v[134:135], v40 offset:8704\n"
    "ds_read_b64_tr_b16 v[136:137], v41\n"
    "ds_read_b64_tr_b16 v[138:139], v41 offset:512\n"
    "ds_read_b64_tr_b16 v[140:141], v41 offset:8192\n"
    "ds_read_b64_tr_b16 v[142:143], v41 offset:8704\n"
    "ds_read_b64_tr_b16 v[144:145], v40 offset:16384\n"
    "ds_read_b64_tr_b16 v[146:147], v40 offset:16896\n"
    "ds_read_b64_tr_b16 v[148:149], v40 offset:24576\n"
    "ds_read_b64_tr_b16 v[150:151], v40 offset:25088\n"
    "ds_read_b64_tr_b16 v[152:153], v41 offset:16384\n"
    "ds_read_b64_tr_b16 v[154:155], v41 offset:16896\n"
    "ds_read_b64_tr_b16 v[156:157], v41 offset:24576\n"
    "ds_read_b64_tr_b16 v[158:159], v41 offset:25088\n"
    "s_branch L_pe8_%=\n"
    "L_pw8_1_%=:\n"
    "s_cmp_lg_u32 s57, 1\n"
    "s_cbranch_scc1 L_pw8_2_%=\n"
    "ds_read_b64_tr_b16 v[160:161], v40\n"
    "ds_read_b64_tr_b16 v[162:163], v40 offset:512\n"
    "ds_read_b64_tr_b16 v[164:165], v40 offset:8192\n"
    "ds_read_b64_tr_b16 v[166:167], v40 offset:8704\n"
    "ds_read_b64_tr_b16 v[168:169], v41\n"
    "ds_read_b64_tr_b16 v[170:171], v41 offset:512\n"
    "ds_read_b64_tr_b16 v[172:173], v41 offset:8192\n"
    "ds_read_b64_tr_b16 v[174:175], v41 offset:8704\n"
    "ds_read_b64_tr_b16 v[176:177], v40 offset:16384\n"
    "ds_read_b64_tr_b16 v[178:179], v40 offset:16896\n"
    "ds_read_b64_tr_b16 v[180:181], v40 offset:24576\n"
    "ds_read_b64_tr_b16 v[182:183], v40 offset:25088\n"
    "ds_read_b64_tr_b16 v[184:185], v41 offset:16384\n"
    "ds_read_b64_tr_b16 v[186:187], v41 offset:16896\n"
    "ds_read_b64_tr_b16 v[188:189], v41 offset:24576\n"
    "ds_read_b64_tr_b16 v[190:191], v41 offset:25088\n"
    "s_branch L_pe8_%=\n"
    "L_pw8_2_%=:\n"
    "s_cmp_lg_u32 s57, 2\n"
    "s_cbranch_scc1 L_pw8_3_%=\n"
    "ds_read_b64_tr_b16 v[192:193], v40\n"
    "ds_read_b64_tr_b16 v[194:195], v40 offset:512\n"
    "ds_read_b64_tr_b16 v[196:197], v40 offset:8192\n"
    "ds_read_b64_tr_b16 v[198:199], v40 offset:8704\n"
    "ds_read_b64_tr_b16 v[200:201], v41\n"
    "ds_read_b64_tr_b16 v[202:203], v41 offset:512\n"
    "ds_read_b64_tr_b16 v[204:205], v41 offset:8192\n"
    "ds_read_b64_tr_b16 v[206:207], v41 offset:8704\n"
    "ds_read_b64_tr_b16 v[208:209], v40 offset:16384\n"
    "ds_read_b64_tr_b16 v[210:211], v40 offset:16896\n"
    "ds_read_b64_tr_b16 v[212:213], v40 offset:24576\n"
    "ds_read_b64_tr_b16 v[214:215], v40 offset:25088\n"
    "ds_read_b64_tr_b16 v[216:217], v41 offset:16384\n"
    "ds_read_b64_tr_b16 v[218:219], v41 offset:16896\n"
    "ds_read_b64_tr_b16 v[220:221], v41 offset:24576\n"
    "ds_read_b64_tr_b16 v[222:223], v41 offset:25088\n"
    "s_branch L_pe8_%=\n"
    "L_pw8_3_%=:\n"
    "ds_read_b64_tr_b16 v[224:225], v40\n"
    "ds_read_b64_tr_b16 v[226:227], v40 offset:512\n"
    "ds_read_b64_tr_b16 v[228:229], v40 offset:8192\n"
    "ds_read_b64_tr_b16 v[230:231], v40 offset:8704\n"
    "ds_read_b64_tr_b16 v[232:233], v41\n"
    "ds_read_b64_tr_b16 v[234:235], v41 offset:512\n"
    "ds_read_b64_tr_b16 v[236:237], v41 offset:8192\n"
    "ds_read_b64_tr_b16 v[238:239], v41 offset:8704\n"
    "ds_read_b64_tr_b16 v[240:241], v40 offset:16384\n"
    "ds_read_b64_tr_b16 v[242:243], v40 offset:16896\n"
    "ds_read_b64_tr_b16 v[244:245], v40 offset:24576\n"
    "ds_read_b64_tr_b16 v[246:247], v40 offset:25088\n"
    "ds_read_b64_tr_b16 v[248:249], v41 offset:16384\n"
    "ds_read_b64_tr_b16 v[250:251], v41 offset:16896\n"
    "ds_read_b64_tr_b16 v[252:253], v41 offset:24576\n"
    "ds_read_b64_tr_b16 v[254:255], v41 offset:25088\n"
    "L_pe8_%=:\n"
    "s_waitcnt lgkmcnt(0)\n"
    "s_barrier\n"
    "s_add_u32 s82, s85, 0\n"
    "s_add_u32 m0, s91, 0\n"
    "s_nop 0\n"
    "buffer_load_dwordx4 v28, s[52:55], s82 offen lds\n"
    "s_add_u32 s82, s85, 1024\n"
    "s_add_u32 m0, s91, 1024\n"
    "s_nop 0\n"
    "buffer_load_dwordx4 v29, s[52:55], s82 offen lds\n"
    "s_add_u32 s82, s85, 2048\n"
    "s_add_u32 m0, s91, 2048\n"
    "s_nop 0\n"
    "buffer_load_dwordx4 v28, s[52:55], s82 offen lds\n"
    "s_add_u32 s82, s85, 3072\n"
    "s_add_u32 m0, s91, 3072\n"
    "s_nop 0\n"
    "buffer_load_dwordx4 v29, s[52:55], s82 offen lds\n"
    "s_add_u32 s82, s85, 4096\n"
    "s_add_u32 m0, s91, 4096\n"
    "s_nop 0\n"
    "buffer_load_dwordx4 v30, s[52:55], s82 offen lds\n"
    "s_add_u32 s82, s85, 5120\n"
    "s_add_u32 m0, s91, 5120\n"
    "s_nop 0\n"
    "buffer_load_dwordx4 v31, s[52:55], s82 offen lds\n"
    "s_add_u32 s82, s85, 6144\n"
    "s_add_u32 m0, s91, 6144\n"
    "s_nop 0\n"
    "buffer_load_dwordx4 v30, s[52:55], s82 offen lds\n"
    "s_add_u32 s82, s85, 7168\n"
    "s_add_u32 m0, s91, 7168\n"
    "s_nop 0\n"
    "buffer_load_dwordx4 v31, s[52:55], s82 offen lds\n"
    "s_waitcnt vmcnt(24)\n"
    "s_barrier\n"
    "s_cmp_lg_u32 s57, 0\n"
    "s_cbranch_scc1 L_pw9_1_%=\n"
    "ds_read_b64_tr_b16 v[160:161], v40 offset:32768\n"
    "ds_read_b64_tr_b16 v[162:163], v40 offset:33280\n"
    "ds_read_b64_tr_b16 v[164:165], v40 offset:40960\n"
    "ds_read_b64_tr_b16 v[166:167], v40 offset:41472\n"
    "ds_read_b64_tr_b16 v[168:169], v41 offset:32768\n"
    "ds_read_b64_tr_b16 v[170:171], v41 offset:33280\n"
    "ds_read_b64_tr_b16 v[172:173], v41 offset:40960\n"
    "ds_read_b64_tr_b16 v[174:175], v41 offset:41472\n"
    "ds_read_b64_tr_b16 v[176:177], v40 offset:49152\n"
    "ds_read_b64_tr_b16 v[178:179], v40 offset:49664\n"
    "ds_read_b64_tr_b16 v[180:181], v40 offset:57344\n"
    "ds_read_b64_tr_b16 v[182:183], v40 offset:57856\n"
    "ds_read_b64_tr_b16 v[184:185], v41 offset:49152\n"
    "ds_read_b64_tr_b16 v[186:187], v41 offset:49664\n"
    "ds_read_b64_tr_b16 v[188:189], v41 offset:57344\n"
    "ds_read_b64_tr_b16 v[190:191], v41 offset:57856\n"
    "s_branch L_pe9_%=\n"
    "L_pw9_1_%=:\n"
    "s_cmp_lg_u32 s57, 1\n"
    "s_cbranch_scc1 L_pw9_2_%=\n"
    "ds_read_b64_tr_b16 v[128:129], v40 offset:32768\n"
    "ds_read_b64_tr_b16 v[130:131], v40 offset:33280\n"
    "ds_read_b64_tr_b16 v[132:133], v40 offset:40960\n"
    "ds_read_b64_tr_b16 v[134:135], v40 offset:41472\n"
    "ds_read_b64_tr_b16 v[136:137], v41 offset:32768\n"
    "ds_read_b64_tr_b16 v[138:139], v41 offset:33280\n"
    "ds_read_b64_tr_b16 v[140:141], v41 offset:40960\n"
    "ds_read_b64_tr_b16 v[142:143], v41 offset:41472\n"
    "ds_read_b64_tr_b16 v[144:145], v40 offset:49152\n"
    "ds_read_b64_tr_b16 v[146:147], v40 offset:49664\n"
    "ds_read_b64_tr_b16 v[148:149], v40 offset:57344\n"
    "ds_read_b64_tr_b16 v[150:151], v40 offset:57856\n"
    "ds_read_b64_tr_b16 v[152:153], v41 offset:49152\n"
    "ds_read_b64_tr_b16 v[154:155], v41 offset:49664\n"
    "ds_read_b64_tr_b16 v[156:157], v41 offset:57344\n"
    "ds_read_b64_tr_b16 v[158:159], v41 offset:57856\n"
    "s_branch L_pe9_%=\n"
    "L_pw9_2_%=:\n"
    "s_cmp_lg_u32 s57, 2\n"
    "s_cbranch_scc1 L_pw9_3_%=\n"
    "ds_read_b64_tr_b16 v[224:225], v40 offset:32768\n"
    "ds_read_b64_tr_b16 v[226:227], v40 offset:33280\n"
    "ds_read_b64_tr_b16 v[228:229], v40 offset:40960\n"
    "ds_read_b64_tr_b16 v[230:231], v40 offset:41472\n"
    "ds_read_b64_tr_b16 v[232:233], v41 offset:32768\n"
    "ds_read_b64_tr_b16 v[234:235], v41 offset:33280\n"
    "ds_read_b64_tr_b16 v[236:237], v41 offset:40960\n"
    "ds_read_b64_tr_b16 v[238:239], v41 offset:41472\n"
    "ds_read_b64_tr_b16 v[240:241], v40 offset:49152\n"
    "ds_read_b64_tr_b16 v[242:243], v40 offset:49664\n"
    "ds_read_b64_tr_b16 v[244:245], v40 offset:57344\n"
    "ds_read_b64_tr_b16 v[246:247], v40 offset:57856\n"
    "ds_read_b64_tr_b16 v[248:249], v41 offset:49152\n"
    "ds_read_b64_tr_b16 v[250:251], v41 offset:49664\n"
    "ds_read_b64_tr_b16 v[252:253], v41 offset:57344\n"
    "ds_read_b64_tr_b16 v[254:255], v41 offset:57856\n"
    "s_branch L_pe9_%=\n"
    "L_pw9_3_%=:\n"
    "ds_read_b64_tr_b16 v[192:193], v40 offset:32768\n"
    "ds_read_b64_tr_b16 v[194:195], v40 offset:33280\n"
    "ds_read_b64_tr_b16 v[196:197], v40 offset:40960\n"
    "ds_read_b64_tr_b16 v[198:199], v40 offset:41472\n"
    "ds_read_b64_tr_b16 v[200:201], v41 offset:32768\n"
    "ds_read_b64_tr_b16 v[202:203], v41 offset:33280\n"
    "ds_read_b64_tr_b16 v[204:205], v41 offset:40960\n"
    "ds_read_b64_tr_b16 v[206:207], v41 offset:41472\n"
    "ds_read_b64_tr_b16 v[208:209], v40 offset:49152\n"
    "ds_read_b64_tr_b16 v[210:211], v40 offset:49664\n"
    "ds_read_b64_tr_b16 v[212:213], v40 offset:57344\n"
    "ds_read_b64_tr_b16 v[214:215], v40 offset:57856\n"
    "ds_read_b64_tr_b16 v[216:217], v41 offset:49152\n"
    "ds_read_b64_tr_b16 v[218:219], v41 offset:49664\n"
    "ds_read_b64_tr_b16 v[220:221], v41 offset:57344\n"
    "ds_read_b64_tr_b16 v[222:223], v41 offset:57856\n"
    "L_pe9_%=:\n"
    "s_waitcnt lgkmcnt(0)\n"
    "s_barrier\n"
    "s_add_u32 s82, s85, 32768\n"
    "s_add_u32 m0, s91, 32768\n"
    "s_nop 0\n"
    "buffer_load_dwordx4 v28, s[52:55], s82 offen lds\n"
    "s_add_u32 s82, s85, 33792\n"
    "s_add_u32 m0, s91, 33792\n"
    "s_nop 0\n"
    "buffer_load_dwordx4 v29, s[52:55], s82 offen lds\n"
    "s_add_u32 s82, s85, 34816\n"
    "s_add_u32 m0, s91, 34816\n"
    "s_nop 0\n"
    "buffer_load_dwordx4 v28, s[52:55], s82 offen lds\n"
    "s_add_u32 s82, s85, 35840\n"
    "s_add_u32 m0, s91, 35840\n"
    "s_nop 0\n"
    "buffer_load_dwordx4 v29, s[52:55], s82 offen lds\n"
    "s_add_u32 s82, s85, 36864\n"
    "s_add_u32 m0, s91, 36864\n"
    "s_nop 0\n"
    "buffer_load_dwordx4 v30, s[52:55], s82 offen lds\n"
    "s_add_u32 s82, s85, 37888\n"
    "s_add_u32 m0, s91, 37888\n"
    "s_nop 0\n"
    "buffer_load_dwordx4 v31, s[52:55], s82 offen lds\n"
    "s_add_u32 s82, s85, 38912\n"
    "s_add_u32 m0, s91, 38912\n"
    "s_nop 0\n"
    "buffer_load_dwordx4 v30, s[52:55], s82 offen lds\n"
    "s_add_u32 s82, s85, 39936\n"
    "s_add_u32 m0, s91, 39936\n"
    "s_nop 0\n"
    "buffer_load_dwordx4 v31, s[52:55], s82 offen lds\n"
    "s_waitcnt vmcnt(24)\n"
    "s_barrier\n"
    "s_cmp_lg_u32 s57, 0\n"
    "s_cbranch_scc1 L_pw10_1_%=\n"
    "ds_read_b64_tr_b16 v[192:193], v44\n"
    "ds_read_b64_tr_b16 v[194:195], v44 offset:512\n"
    "ds_read_b64_tr_b16 v[196:197], v44 offset:8192\n"
    "ds_read_b64_tr_b16 v[198:199], v44 offset:8704\n"
    "ds_read_b64_tr_b16 v[200:201], v45\n"
    "ds_read_b64_tr_b16 v[202:203], v45 offset:512\n"
    "ds_read_b64_tr_b16 v[204:205], v45 offset:8192\n"
    "ds_read_b64_tr_b16 v[206:207], v45 offset:8704\n"
    "ds_read_b64_tr_b16 v[208:209], v44 offset:16384\n"
    "ds_read_b64_tr_b16 v[210:211], v44 offset:16896\n"
    "ds_read_b64_tr_b16 v[212:213], v44 offset:24576\n"
    "ds_read_b64_tr_b16 v[214:215], v44 offset:25088\n"
    "ds_read_b64_tr_b16 v[216:217], v45 offset:16384\n"
    "ds_read_b64_tr_b16 v[218:219], v45 offset:16896\n"
    "ds_read_b64_tr_b16 v[220:221], v45 offset:24576\n"
    "ds_read_b64_tr_b16 v[222:223], v45 offset:25088\n"
    "s_branch L_pe10_%=\n"
    "L_pw10_1_%=:\n"
    "s_cmp_lg_u32 s57, 1\n"
    "s_cbranch_scc1 L_pw10_2_%=\n"
    "ds_read_b64_tr_b16 v[224:225], v44\n"
    "ds_read_b64_tr_b16 v[226:227], v44 offset:512\n"
    "ds_read_b64_tr_b16 v[228:229], v44 offset:8192\n"
    "ds_read_b64_tr_b16 v[230:231], v44 offset:8704\n"
    "ds_read_b64_tr_b16 v[232:233], v45\n"
    "ds_read_b64_tr_b16 v[234:235], v45 offset:512\n"
    "ds_read_b64_tr_b16 v[236:237], v45 offset:8192\n"
    "ds_read_b64_tr_b16 v[238:239], v45 offset:8704\n"
    "ds_read_b64_tr_b16 v[240:241], v44 offset:16384\n"
    "ds_read_b64_tr_b16 v[242:243], v44 offset:16896\n"
    "ds_read_b64_tr_b16 v[244:245], v44 offset:24576\n"
    "ds_read_b64_tr_b16 v[246:247], v44 offset:25088\n"
    "ds_read_b64_tr_b16 v[248:249], v45 offset:16384\n"
    "ds_read_b64_tr_b16 v[250:251], v45 offset:16896\n"
    "ds_read_b64_tr_b16 v[252:253], v45 offset:24576\n"
    "ds_read_b64_tr_b16 v[254:255], v45 offset:25088\n"
    "s_branch L_pe10_%=\n"
    "L_pw10_2_%=:\n"
    "s_cmp_lg_u32 s57, 2\n"
    "s_cbranch_scc1 L_pw10_3_%=\n"
    "ds_read_b64_tr_b16 v[128:129], v44\n"
    "ds_read_b64_tr_b16 v[130:131], v44 offset:512\n"
    "ds_read_b64_tr_b16 v[132:133], v44 offset:8192\n"
    "ds_read_b64_tr_b16 v[134:135], v44 offset:8704\n"
    "ds_read_b64_tr_b16 v[136:137], v45\n"
    "ds_read_b64_tr_b16 v[138:139], v45 offset:512\n"
    "ds_read_b64_tr_b16 v[140:141], v45 offset:8192\n"
    "ds_read_b64_tr_b16 v[142:143], v45 offset:8704\n"
    "ds_read_b64_tr_b16 v[144:145], v44 offset:16384\n"
    "ds_read_b64_tr_b16 v[146:147], v44 offset:16896\n"
    "ds_read_b64_tr_b16 v[148:149], v44 offset:24576\n"
    "ds_read_b64_tr_b16 v[150:151], v44 offset:25088\n"
    "ds_read_b64_tr_b16 v[152:153], v45 offset:16384\n"
    "ds_read_b64_tr_b16 v[154:155], v45 offset:16896\n"
    "ds_read_b64_tr_b16 v[156:157], v45 offset:24576\n"
    "ds_read_b64_tr_b16 v[158:159], v45 offset:25088\n"
    "s_branch L_pe10_%=\n"
    "L_pw10_3_%=:\n"
    "ds_read_b64_tr_b16 v[160:161], v44\n"
    "ds_read_b64_tr_b16 v[162:163], v44 offset:512\n"
    "ds_read_b64_tr_b16 v[164:165], v44 offset:8192\n"
    "ds_read_b64_tr_b16 v[166:167], v44 offset:8704\n"
    "ds_read_b64_tr_b16 v[168:169], v45\n"
    "ds_read_b64_tr_b16 v[170:171], v45 offset:512\n"
    "ds_read_b64_tr_b16 v[172:173], v45 offset:8192\n"
    "ds_read_b64_tr_b16 v[174:175], v45 offset:8704\n"
    "ds_read_b64_tr_b16 v[176:177], v44 offset:16384\n"
    "ds_read_b64_tr_b16 v[178:179], v44 offset:16896\n"
    "ds_read_b64_tr_b16 v[180:181], v44 offset:24576\n"
    "ds_read_b64_tr_b16 v[182:183], v44 offset:25088\n"
    "ds_read_b64_tr_b16 v[184:185], v45 offset:16384\n"
    "ds_read_b64_tr_b16 v[186:187], v45 offset:16896\n"
    "ds_read_b64_tr_b16 v[188:189], v45 offset:24576\n"
    "ds_read_b64_tr_b16 v[190:191], v45 offset:25088\n"
    "L_pe10_%=:\n"
    "s_waitcnt lgkmcnt(0)\n"
    "s_barrier\n"
    "s_add_u32 s82, s85, 65536\n"
    "s_add_u32 m0, s91, 65536\n"
    "s_nop 0\n"
    "buffer_load_dwordx4 v28, s[52:55], s82 offen lds\n"
    "s_add_u32 s82, s85, 66560\n"
    "s_add_u32 m0, s91, 66560\n"
    "s_nop 0\n"
    "buffer_load_dwordx4 v29, s[52:55], s82 offen lds\n"
    "s_add_u32 s82, s85, 67584\n"
    "s_add_u32 m0, s91, 67584\n"
    "s_nop 0\n"
    "buffer_load_dwordx4 v28, s[52:55], s82 offen lds\n"
    "s_add_u32 s82, s85, 68608\n"
    "s_add_u32 m0, s91, 68608\n"
    "s_nop 0\n"
    "buffer_load_dwordx4 v29, s[52:55], s82 offen lds\n"
    "s_add_u32 s82, s85, 69632\n"
    "s_add_u32 m0, s91, 69632\n"
    "s_nop 0\n"
    "buffer_load_dwordx4 v30, s[52:55], s82 offen lds\n"
    "s_add_u32 s82, s85, 70656\n"
    "s_add_u32 m0, s91, 70656\n"
    "s_nop 0\n"
    "buffer_load_dwordx4 v31, s[52:55], s82 offen lds\n"
    "s_add_u32 s82, s85, 71680\n"
    "s_add_u32 m0, s91, 71680\n"
    "s_nop 0\n"
    "buffer_load_dwordx4 v30, s[52:55], s82 offen lds\n"
    "s_add_u32 s82, s85, 72704\n"
    "s_add_u32 m0, s91, 72704\n"
    "s_nop 0\n"
    "buffer_load_dwordx4 v31, s[52:55], s82 offen lds\n"
    "s_waitcnt vmcnt(24)\n"
    "s_barrier\n"
    "s_cmp_lg_u32 s57, 0\n"
    "s_cbranch_scc1 L_pw11_1_%=\n"
    "ds_read_b64_tr_b16 v[224:225], v44 offset:32768\n"
    "ds_read_b64_tr_b16 v[226:227], v44 offset:33280\n"
    "ds_read_b64_tr_b16 v[228:229], v44 offset:40960\n"
    "ds_read_b64_tr_b16 v[230:231], v44 offset:41472\n"
    "ds_read_b64_tr_b16 v[232:233], v45 offset:32768\n"
    "ds_read_b64_tr_b16 v[234:235], v45 offset:33280\n"
    "ds_read_b64_tr_b16 v[236:237], v45 offset:40960\n"
    "ds_read_b64_tr_b16 v[238:239], v45 offset:41472\n"
    "ds_read_b64_tr_b16 v[240:241], v44 offset:49152\n"
    "ds_read_b64_tr_b16 v[242:243], v44 offset:49664\n"
    "ds_read_b64_tr_b16 v[244:245], v44 offset:57344\n"
    "ds_read_b64_tr_b16 v[246:247], v44 offset:57856\n"
    "ds_read_b64_tr_b16 v[248:249], v45 offset:49152\n"
    "ds_read_b64_tr_b16 v[250:251], v45 offset:49664\n"
    "ds_read_b64_tr_b16 v[252:253], v45 offset:57344\n"
    "ds_read_b64_tr_b16 v[254:255], v45 offset:57856\n"
    "s_branch L_pe11_%=\n"
    "L_pw11_1_%=:\n"
    "s_cmp_lg_u32 s57, 1\n"
    "s_cbranch_scc1 L_pw11_2_%=\n"
    "ds_read_b64_tr_b16 v[192:193], v44 offset:32768\n"
    "ds_read_b64_tr_b16 v[194:195], v44 offset:33280\n"
    "ds_read_b64_tr_b16 v[196:197], v44 offset:40960\n"
    "ds_read_b64_tr_b16 v[198:199], v44 offset:41472\n"
    "ds_read_b64_tr_b16 v[200:201], v45 offset:32768\n"
    "ds_read_b64_tr_b16 v[202:203], v45 offset:33280\n"
    "ds_read_b64_tr_b16 v[204:205], v45 offset:40960\n"
    "ds_read_b64_tr_b16 v[206:207], v45 offset:41472\n"
    "ds_read_b64_tr_b16 v[208:209], v44 offset:49152\n"
    "ds_read_b64_tr_b16 v[210:211], v44 offset:49664\n"
    "ds_read_b64_tr_b16 v[212:213], v44 offset:57344\n"
    "ds_read_b64_tr_b16 v[214:215], v44 offset:57856\n"
    "ds_read_b64_tr_b16 v[216:217], v45 offset:49152\n"
    "ds_read_b64_tr_b16 v[218:219], v45 offset:49664\n"
    "ds_read_b64_tr_b16 v[220:221], v45 offset:57344\n"
    "ds_read_b64_tr_b16 v[222:223], v45 offset:57856\n"
    "s_branch L_pe11_%=\n"
    "L_pw11_2_%=:\n"
    "s_cmp_lg_u32 s57, 2\n"
    "s_cbranch_scc1 L_pw11_3_%=\n"
    "ds_read_b64_tr_b16 v[160:161], v44 offset:32768\n"
    "ds_read_b64_tr_b16 v[162:163], v44 offset:33280\n"
    "ds_read_b64_tr_b16 v[164:165], v44 offset:40960\n"
    "ds_read_b64_tr_b16 v[166:167], v44 offset:41472\n"
    "ds_read_b64_tr_b16 v[168:169], v45 offset:32768\n"
    "ds_read_b64_tr_b16 v[170:171], v45 offset:33280\n"
    "ds_read_b64_tr_b16 v[172:173], v45 offset:40960\n"
    "ds_read_b64_tr_b16 v[174:175], v45 offset:41472\n"
    "ds_read_b64_tr_b16 v[176:177], v44 offset:49152\n"
    "ds_read_b64_tr_b16 v[178:179], v44 offset:49664\n"
    "ds_read_b64_tr_b16 v[180:181], v44 offset:57344\n"
    "ds_read_b64_tr_b16 v[182:183], v44 offset:57856\n"
    "ds_read_b64_tr_b16 v[184:185], v45 offset:49152\n"
    "ds_read_b64_tr_b16 v[186:187], v45 offset:49664\n"
    "ds_read_b64_tr_b16 v[188:189], v45 offset:57344\n"
    "ds_read_b64_tr_b16 v[190:191], v45 offset:57856\n"
    "s_branch L_pe11_%=\n"
    "L_pw11_3_%=:\n"
    "ds_read_b64_tr_b16 v[128:129], v44 offset:32768\n"
    "ds_read_b64_tr_b16 v[130:131], v44 offset:33280\n"
    "ds_read_b64_tr_b16 v[132:133], v44 offset:40960\n"
    "ds_read_b64_tr_b16 v[134:135], v44 offset:41472\n"
    "ds_read_b64_tr_b16 v[136:137], v45 offset:32768\n"
    "ds_read_b64_tr_b16 v[138:139], v45 offset:33280\n"
    "ds_read_b64_tr_b16 v[140:141], v45 offset:40960\n"
    "ds_read_b64_tr_b16 v[142:143], v45 offset:41472\n"
    "ds_read_b64_tr_b16 v[144:145], v44 offset:49152\n"
    "ds_read_b64_tr_b16 v[146:147], v44 offset:49664\n"
    "ds_read_b64_tr_b16 v[148:149], v44 offset:57344\n"
    "ds_read_b64_tr_b16 v[150:151], v44 offset:57856\n"
    "ds_read_b64_tr_b16 v[152:153], v45 offset:49152\n"
    "ds_read_b64_tr_b16 v[154:155], v45 offset:49664\n"
    "ds_read_b64_tr_b16 v[156:157], v45 offset:57344\n"
    "ds_read_b64_tr_b16 v[158:159], v45 offset:57856\n"
    "L_pe11_%=:\n"
    "s_waitcnt lgkmcnt(0)\n"
    "s_barrier\n"
    "s_add_u32 s82, s85, 98304\n"
    "s_add_u32 m0, s91, 98304\n"
    "s_nop 0\n"
    "buffer_load_dwordx4 v28, s[52:55], s82 offen lds\n"
    "s_add_u32 s82, s85, 99328\n"
    "s_add_u32 m0, s91, 99328\n"
    "s_nop 0\n"
    "buffer_load_dwordx4 v29, s[52:55], s82 offen lds\n"
    "s_add_u32 s82, s85, 100352\n"
    "s_add_u32 m0, s91, 100352\n"
    "s_nop 0\n"
    "buffer_load_dwordx4 v28, s[52:55], s82 offen lds\n"
    "s_add_u32 s82, s85, 101376\n"
    "s_add_u32 m0, s91, 101376\n"
    "s_nop 0\n"
    "buffer_load_dwordx4 v29, s[52:55], s82 offen lds\n"
    "s_add_u32 s82, s85, 102400\n"
    "s_add_u32 m0, s91, 102400\n"
    "s_nop 0\n"
    "buffer_load_dwordx4 v30, s[52:55], s82 offen lds\n"
    "s_add_u32 s82, s85, 103424\n"
    "s_add_u32 m0, s91, 103424\n"
    "s_nop 0\n"
    "buffer_load_dwordx4 v31, s[52:55], s82 offen lds\n"
    "s_add_u32 s82, s85, 104448\n"
    "s_add_u32 m0, s91, 104448\n"
    "s_nop 0\n"
    "buffer_load_dwordx4 v30, s[52:55], s82 offen lds\n"
    "s_add_u32 s82, s85, 105472\n"
    "s_add_u32 m0, s91, 105472\n"
    "s_nop 0\n"
    "buffer_load_dwordx4 v31, s[52:55], s82 offen lds\n"
    "s_waitcnt vmcnt(24)\n"
    "s_barrier\n"
    "ds_read_b64_tr_b16 v[64:65], v48\n"
    "ds_read_b64_tr_b16 v[66:67], v48 offset:2048\n"
    "ds_read_b64_tr_b16 v[68:69], v49\n"
    "ds_read_b64_tr_b16 v[70:71], v49 offset:2048\n"
    "ds_read_b64_tr_b16 v[72:73], v50\n"
    "ds_read_b64_tr_b16 v[74:75], v50 offset:2048\n"
    "ds_read_b64_tr_b16 v[76:77], v51\n"
    "ds_read_b64_tr_b16 v[78:79], v51 offset:2048\n"
    "ds_read_b64_tr_b16 v[80:81], v52\n"
    "ds_read_b64_tr_b16 v[82:83], v52 offset:2048\n"
    "ds_read_b64_tr_b16 v[84:85], v53\n"
    "ds_read_b64_tr_b16 v[86:87], v53 offset:2048\n"
    "ds_read_b64_tr_b16 v[88:89], v54\n"
    "ds_read_b64_tr_b16 v[90:91], v54 offset:2048\n"
    "ds_read_b64_tr_b16 v[92:93], v55\n"
    "ds_read_b64_tr_b16 v[94:95], v55 offset:2048\n"
    "s_waitcnt lgkmcnt(0)\n"
    "s_barrier\n"
    "ds_write_b128 v9, v[64:67]\n"
    "ds_write_b128 v9, v[68:71] offset:1024\n"
    "ds_write_b128 v9, v[72:75] offset:2048\n"
    "ds_write_b128 v9, v[76:79] offset:3072\n"
    "ds_write_b128 v9, v[80:83] offset:4096\n"
    "ds_write_b128 v9, v[84:87] offset:5120\n"
    "ds_write_b128 v9, v[88:91] offset:6144\n"
    "ds_write_b128 v9, v[92:95] offset:7168\n"
    "s_waitcnt lgkmcnt(0)\n"
    "s_waitcnt vmcnt(16)\n"
    "s_barrier\n"
    "ds_read_b64_tr_b16 v[64:65], v48 offset:32768\n"
    "ds_read_b64_tr_b16 v[66:67], v48 offset:34816\n"
    "ds_read_b64_tr_b16 v[68:69], v49 offset:32768\n"
    "ds_read_b64_tr_b16 v[70:71], v49 offset:34816\n"
    "ds_read_b64_tr_b16 v[72:73], v50 offset:32768\n"
    "ds_read_b64_tr_b16 v[74:75], v50 offset:34816\n"
    "ds_read_b64_tr_b16 v[76:77], v51 offset:32768\n"
    "ds_read_b64_tr_b16 v[78:79], v51 offset:34816\n"
    "ds_read_b64_tr_b16 v[80:81], v52 offset:32768\n"
    "ds_read_b64_tr_b16 v[82:83], v52 offset:34816\n"
    "ds_read_b64_tr_b16 v[84:85], v53 offset:32768\n"
    "ds_read_b64_tr_b16 v[86:87], v53 offset:34816\n"
    "ds_read_b64_tr_b16 v[88:89], v54 offset:32768\n"
    "ds_read_b64_tr_b16 v[90:91], v54 offset:34816\n"
    "ds_read_b64_tr_b16 v[92:93], v55 offset:32768\n"
    "ds_read_b64_tr_b16 v[94:95], v55 offset:34816\n"
    "s_waitcnt lgkmcnt(0)\n"
    "s_barrier\n"
    "ds_write_b128 v9, v[64:67] offset:32768\n"
    "ds_write_b128 v9, v[68:71] offset:33792\n"
    "ds_write_b128 v9, v[72:75] offset:34816\n"
    "ds_write_b128 v9, v[76:79] offset:35840\n"
    "ds_write_b128 v9, v[80:83] offset:36864\n"
    "ds_write_b128 v9, v[84:87] offset:37888\n"
    "ds_write_b128 v9, v[88:91] offset:38912\n"
    "ds_write_b128 v9, v[92:95] offset:39936\n"
    "s_waitcnt lgkmcnt(0)\n"
    "s_waitcnt vmcnt(8)\n"
    "s_barrier\n"
    "ds_read_b64_tr_b16 v[64:65], v56\n"
    "ds_read_b64_tr_b16 v[66:67], v56 offset:2048\n"
    "ds_read_b64_tr_b16 v[68:69], v57\n"
    "ds_read_b64_tr_b16 v[70:71], v57 offset:2048\n"
    "ds_read_b64_tr_b16 v[72:73], v58\n"
    "ds_read_b64_tr_b16 v[74:75], v58 offset:2048\n"
    "ds_read_b64_tr_b16 v[76:77], v59\n"
    "ds_read_b64_tr_b16 v[78:79], v59 offset:2048\n"
    "ds_read_b64_tr_b16 v[80:81], v60\n"
    "ds_read_b64_tr_b16 v[82:83], v60 offset:2048\n"
    "ds_read_b64_tr_b16 v[84:85], v61\n"
    "ds_read_b64_tr_b16 v[86:87], v61 offset:2048\n"
    "ds_read_b64_tr_b16 v[88:89], v62\n"
    "ds_read_b64_tr_b16 v[90:91], v62 offset:2048\n"
    "ds_read_b64_tr_b16 v[92:93], v63\n"
    "ds_read_b64_tr_b16 v[94:95], v63 offset:2048\n"
    "s_waitcnt lgkmcnt(0)\n"
    "s_barrier\n"
    "ds_write_b128 v10, v[64:67]\n"
    "ds_write_b128 v10, v[68:71] offset:1024\n"
    "ds_write_b128 v10, v[72:75] offset:2048\n"
    "ds_write_b128 v10, v[76:79] offset:3072\n"
    "ds_write_b128 v10, v[80:83] offset:4096\n"
    "ds_write_b128 v10, v[84:87] offset:5120\n"
    "ds_write_b128 v10, v[88:91] offset:6144\n"
    "ds_write_b128 v10, v[92:95] offset:7168\n"
    "s_waitcnt lgkmcnt(0)\n"
    "s_waitcnt vmcnt(0)\n"
    "s_barrier\n"
    "ds_read_b64_tr_b16 v[64:65], v56 offset:32768\n"
    "ds_read_b64_tr_b16 v[66:67], v56 offset:34816\n"
    "ds_read_b64_tr_b16 v[68:69], v57 offset:32768\n"
    "ds_read_b64_tr_b16 v[70:71], v57 offset:34816\n"
    "ds_read_b64_tr_b16 v[72:73], v58 offset:32768\n"
    "ds_read_b64_tr_b16 v[74:75], v58 offset:34816\n"
    "ds_read_b64_tr_b16 v[76:77], v59 offset:32768\n"
    "ds_read_b64_tr_b16 v[78:79], v59 offset:34816\n"
    "ds_read_b64_tr_b16 v[80:81], v60 offset:32768\n"
    "ds_read_b64_tr_b16 v[82:83], v60 offset:34816\n"
    "ds_read_b64_tr_b16 v[84:85], v61 offset:32768\n"
    "ds_read_b64_tr_b16 v[86:87], v61 offset:34816\n"
    "ds_read_b64_tr_b16 v[88:89], v62 offset:32768\n"
    "ds_read_b64_tr_b16 v[90:91], v62 offset:34816\n"
    "ds_read_b64_tr_b16 v[92:93], v63 offset:32768\n"
    "ds_read_b64_tr_b16 v[94:95], v63 offset:34816\n"
    "s_waitcnt lgkmcnt(0)\n"
    "s_barrier\n"
    "ds_write_b128 v10, v[64:67] offset:32768\n"
    "ds_write_b128 v10, v[68:71] offset:33792\n"
    "ds_write_b128 v10, v[72:75] offset:34816\n"
    "ds_write_b128 v10, v[76:79] offset:35840\n"
    "ds_write_b128 v10, v[80:83] offset:36864\n"
    "ds_write_b128 v10, v[84:87] offset:37888\n"
    "ds_write_b128 v10, v[88:91] offset:38912\n"
    "ds_write_b128 v10, v[92:95] offset:39936\n"
    "s_waitcnt lgkmcnt(0)\n"
    "v_readfirstlane_b32 s82, v96\n"
    "v_readfirstlane_b32 s83, v97\n"
    "v_readfirstlane_b32 s84, v98\n"
    "v_readfirstlane_b32 s85, v99\n"
    "s_nop 1\n"
    "s_cmp_lt_u32 s83, s82\n"
    "s_cselect_b32 s88, 8192, 0\n"
    "s_cmp_lt_u32 s85, s84\n"
    "s_cselect_b32 s89, 8192, 0\n"
    "s_add_u32 s64, s88, 0\n"
    "s_xor_b32 s65, s64, 524288\n"
    "s_lshr_b32 s83, s56, 3\n"
    "s_add_u32 s82, s83, 0\n"
    "s_and_b32 s82, s82, 3\n"
    "s_lshl_b32 s82, s82, 15\n"
    "s_add_u32 s82, s82, 0\n"
    "s_add_u32 s66, s64, s82\n"
    "s_add_u32 s82, s83, 1\n"
    "s_and_b32 s82, s82, 3\n"
    "s_lshl_b32 s82, s82, 15\n"
    "s_add_u32 s82, s82, 512\n"
    "s_add_u32 s67, s64, s82\n"
    "s_add_u32 s82, s83, 2\n"
    "s_and_b32 s82, s82, 3\n"
    "s_lshl_b32 s82, s82, 15\n"
    "s_add_u32 s82, s82, 1024\n"
    "s_add_u32 s68, s64, s82\n"
    "s_add_u32 s82, s83, 3\n"
    "s_and_b32 s82, s82, 3\n"
    "s_lshl_b32 s82, s82, 15\n"
    "s_add_u32 s82, s82, 1536\n"
    "s_add_u32 s69, s64, s82\n"
    "s_add_u32 s70, s89, 2113536\n"
    "s_add_u32 s71, s89, 2097152\n"
    "s_xor_b32 s76, s76, s89\n"
    "s_xor_b32 s77, s77, s89\n"
    "v_xor_b32 v16, s89, v16\n"
    "s_lshl_b32 s82, s57, 11\n"
    "s_add_u32 s82, s82, s58\n"
    "s_add_u32 m0, s82, 14336\n"
    "s_mov_b32 s62, -1\n"
    "s_mov_b32 s84, 0\n"
    "s_branch L_kvw0_%=\n"
    "L_kvok0_%=:\n"
    "buffer_load_dwordx4 v14, s[36:39], s72 offen offset:0 sc1 lds\n"
    "buffer_load_dwordx4 v14, s[36:39], s72 offen offset:1024 sc1 lds\n"
    "buffer_load_dwordx4 v[24:27], v14, s[36:39], s73 offen sc1\n"
    "buffer_load_dwordx4 v[28:31], v14, s[36:39], s73 offen offset:1024 sc1\n"
    "s_waitcnt vmcnt(0)\n"
    "v_mov_b64 v[64:65], 0\n"
    "v_mov_b64 v[66:67], 0\n"
    "v_mov_b64 v[68:69], 0\n"
    "v_mov_b64 v[70:71], 0\n"
    "v_mov_b64 v[72:73], 0\n"
    "v_mov_b64 v[74:75], 0\n"
    "v_mov_b64 v[76:77], 0\n"
    "v_mov_b64 v[78:79], 0\n"
    "v_mov_b64 v[80:81], 0\n"
    "v_mov_b64 v[82:83], 0\n"
    "v_mov_b64 v[84:85], 0\n"
    "v_mov_b64 v[86:87], 0\n"
    "s_nop 3\n"
    ".p2align 5\n"
    "L_al_%=:\n"
    "s_nop 0\n"
    "s_nop 0\n"
    "s_nop 0\n"
    "s_nop 0\n"
    "L_top_%=:\n"
    "ds_write_b128 v2, v[32:35]\n"
    "ds_write_b128 v2, v[36:39] offset:1024\n"
    "v_smfmac_f32_16x16x64_bf16 v[64:67], v[32:35], a[0:7], v1\n"
    "v_smfmac_f32_16x16x64_bf16 v[68:71], v[32:35], a[8:15], v1\n"
    "v_smfmac_f32_16x16x64_bf16 v[72:75], v[32:35], a[128:135], v1\n"
    "v_smfmac_f32_16x16x64_bf16 v[76:79], v[32:35], a[136:143], v1\n"
    "s_waitcnt lgkmcnt(0)\n"
    "s_barrier\n"
    "v_smfmac_f32_16x16x64_bf16 v[64:67], v[36:39], a[16:23], v1\n"
    "ds_read_b128 v[40:43], v3\n"
    "ds_read_b128 v[44:47], v3 offset:1024\n"
    "v_smfmac_f32_16x16x64_bf16 v[68:71], v[36:39], a[24:31], v1\n"
    "ds_read_b128 v[48:51], v4\n"
    "ds_read_b128 v[52:55], v4 offset:1024\n"
    "v_smfmac_f32_16x16x64_bf16 v[72:75], v[36:39], a[144:151], v1\n"
    "ds_read_b128 v[56:59], v5\n"
    "ds_read_b128 v[60:63], v5 offset:1024\n"
    "v_smfmac_f32_16x16x64_bf16 v[76:79], v[36:39], a[152:159], v1\n"
    "s_waitcnt lgkmcnt(5)\n"
    "v_smfmac_f32_16x16x64_bf16 v[64:67], v[40:43], a[32:39], v1\n"
    "s_sub_u32 s82, s60, 1\n"
    "v_mov_b32 v124, s82\n"
    "v_smfmac_f32_16x16x64_bf16 v[68:71], v[40:43], a[40:47], v1\n"
    "buffer_store_dword v124, v16, s[36:39], 0 offen sc1\n"
    "ds_read_b128 v[112:115], v0 offset:20480\n"
    "s_waitcnt lgkmcnt(5)\n"
    "v_smfmac_f32_16x16x64_bf16 v[64:67], v[44:47], a[48:55], v1\n"
    "ds_read_b128 v[116:119], v0 offset:21504\n"
    "ds_read_b128 v[88:91], v0 offset:14336\n"
    "v_smfmac_f32_16x16x64_bf16 v[68:71], v[44:47], a[56:63], v1\n"
    "ds_read_b128 v[92:95], v0 offset:15360\n"
    "ds_read_b128 v[104:107], v0 offset:18432\n"
    "s_waitcnt lgkmcnt(8)\n"
    "v_smfmac_f32_16x16x64_bf16 v[64:67], v[48:51], a[64:71], v1\n"
    "ds_read_b128 v[108:111], v0 offset:19456\n"
    "ds_read_b128 v[96:99], v0 offset:16384\n"
    "v_smfmac_f32_16x16x64_bf16 v[68:71], v[48:51], a[72:79], v1\n"
    "ds_read_b128 v[100:103], v0 offset:17408\n"
    "s_waitcnt lgkmcnt(10)\n"
    "v_smfmac_f32_16x16x64_bf16 v[64:67], v[52:55], a[80:87], v1\n"
    "v_smfmac_f32_16x16x64_bf16 v[68:71], v[52:55], a[88:95], v1\n"
    "s_waitcnt lgkmcnt(9)\n"
    "v_smfmac_f32_16x16x64_bf16 v[64:67], v[56:59], a[96:103], v1\n"
    "v_smfmac_f32_16x16x64_bf16 v[68:71], v[56:59], a[104:111], v1\n"
    "s_waitcnt lgkmcnt(8)\n"
    "v_smfmac_f32_16x16x64_bf16 v[64:67], v[60:63], a[112:119], v1\n"
    "v_smfmac_f32_16x16x64_bf16 v[68:71], v[60:63], a[120:127], v1\n"
    "v_smfmac_f32_16x16x64_bf16 v[72:75], v[40:43], a[160:167], v1\n"
    "v_smfmac_f32_16x16x64_bf16 v[76:79], v[40:43], a[168:175], v1\n"
    "v_smfmac_f32_16x16x64_bf16 v[72:75], v[44:47], a[176:183], v1\n"
    "v_smfmac_f32_16x16x64_bf16 v[76:79], v[44:47], a[184:191], v1\n"
    "v_smfmac_f32_16x16x64_bf16 v[72:75], v[48:51], a[192:199], v1\n"
    "s_nop 2\n"
    "v_permlane32_swap_b32 v64, v68\n"
    "v_permlane32_swap_b32 v65, v69\n"
    "v_smfmac_f32_16x16x64_bf16 v[76:79], v[48:51], a[200:207], v1\n"
    "v_permlane32_swap_b32 v66, v70\n"
    "v_permlane32_swap_b32 v67, v71\n"
    "v_smfmac_f32_16x16x64_bf16 v[72:75], v[52:55], a[208:215], v1\n"
    "v_pk_add_f32 v[64:65], v[64:65], v[68:69]\n"
    "v_pk_add_f32 v[66:67], v[66:67], v[70:71]\n"
    "v_smfmac_f32_16x16x64_bf16 v[76:79], v[52:55], a[216:223], v1\n"
    "v_cvt_pk_bf16_f32 v68, v64, v65\n"
    "v_cvt_pk_bf16_f32 v69, v66, v67\n"
    "v_smfmac_f32_16x16x64_bf16 v[72:75], v[56:59], a[224:231], v1\n"
    "ds_write_b64 v6, v[68:69] offset:8192\n"
    "v_smfmac_f32_16x16x64_bf16 v[76:79], v[56:59], a[232:239], v1\n"
    "v_smfmac_f32_16x16x64_bf16 v[72:75], v[60:63], a[240:247], v1\n"
    "v_smfmac_f32_16x16x64_bf16 v[76:79], v[60:63], a[248:255], v1\n"
    "v_smfmac_f32_16x16x64_bf16 v[80:83], v[32:35], v[128:135], v1\n"
    "v_smfmac_f32_16x16x64_bf16 v[84:87], v[32:35], v[136:143], v1\n"
    "v_smfmac_f32_16x16x64_bf16 v[80:83], v[36:39], v[144:151], v1\n"
    "s_nop 4\n"
    "v_permlane32_swap_b32 v72, v76\n"
    "v_permlane32_swap_b32 v73, v77\n"
    "v_smfmac_f32_16x16x64_bf16 v[84:87], v[36:39], v[152:159], v1\n"
    "v_permlane32_swap_b32 v74, v78\n"
    "v_permlane32_swap_b32 v75, v79\n"
    "v_smfmac_f32_16x16x64_bf16 v[80:83], v[40:43], v[160:167], v1\n"
    "v_pk_add_f32 v[72:73], v[72:73], v[76:77]\n"
    "v_pk_add_f32 v[74:75], v[74:75], v[78:79]\n"
    "v_smfmac_f32_16x16x64_bf16 v[84:87], v[40:43], v[168:175], v1\n"
    "v_cvt_pk_bf16_f32 v76, v72, v73\n"
    "v_cvt_pk_bf16_f32 v77, v74, v75\n"
    "v_smfmac_f32_16x16x64_bf16 v[80:83], v[44:47], v[176:183], v1\n"
    "ds_write_b64 v6, v[76:77] offset:10240\n"
    "buffer_store_dwordx2 v[76:77], v15, s[36:39], s74 offen sc1\n"
    "v_smfmac_f32_16x16x64_bf16 v[84:87], v[44:47], v[184:191], v1\n"
    "s_waitcnt lgkmcnt(0)\n"
    "s_barrier\n"
    "v_smfmac_f32_16x16x64_bf16 v[80:83], v[48:51], v[192:199], v1\n"
    "ds_read_b64_tr_b16 v[64:65], v7 offset:8192\n"
    "ds_read_b64_tr_b16 v[66:67], v7 offset:8448\n"
    "v_smfmac_f32_16x16x64_bf16 v[84:87], v[48:51], v[200:207], v1\n"
    "ds_read_b64_tr_b16 v[68:69], v7 offset:8704\n"
    "ds_read_b64_tr_b16 v[70:71], v7 offset:8960\n"
    "v_smfmac_f32_16x16x64_bf16 v[80:83], v[52:55], v[208:215], v1\n"
    "ds_read_b64_tr_b16 v[120:121], v7 offset:9216\n"
    "ds_read_b64_tr_b16 v[122:123], v7 offset:9472\n"
    "v_smfmac_f32_16x16x64_bf16 v[84:87], v[52:55], v[216:223], v1\n"
    "ds_read_b64_tr_b16 v[124:125], v7 offset:9728\n"
    "ds_read_b64_tr_b16 v[126:127], v7 offset:9984\n"
    "v_smfmac_f32_16x16x64_bf16 v[80:83], v[56:59], v[224:231], v1\n"
    "ds_read_b64_tr_b16 v[72:73], v7 offset:10240\n"
    "ds_read_b64_tr_b16 v[74:75], v7 offset:10496\n"
    "v_smfmac_f32_16x16x64_bf16 v[84:87], v[56:59], v[232:239], v1\n"
    "ds_read_b64_tr_b16 v[76:77], v7 offset:10752\n"
    "ds_read_b64_tr_b16 v[78:79], v7 offset:11008\n"
    "v_smfmac_f32_16x16x64_bf16 v[80:83], v[60:63], v[240:247], v1\n"
    "ds_read_b64_tr_b16 v[32:33], v7 offset:11264\n"
    "ds_read_b64_tr_b16 v[34:35], v7 offset:11520\n"
    "v_smfmac_f32_16x16x64_bf16 v[84:87], v[60:63], v[248:255], v1\n"
    "ds_read_b64_tr_b16 v[36:37], v7 offset:11776\n"
    "ds_read_b64_tr_b16 v[38:39], v7 offset:12032\n"
    "s_waitcnt lgkmcnt(14)\n"
    "v_mfma_f32_16x16x32_bf16 v[52:55], v[88:91], v[64:67], 0\n"
    "s_waitcnt lgkmcnt(12)\n"
    "v_mfma_f32_16x16x32_bf16 v[52:55], v[92:95], v[68:71], v[52:55]\n"
    "s_waitcnt lgkmcnt(10)\n"
    "v_mfma_f32_16x16x32_bf16 v[52:55], v[96:99], v[120:123], v[52:55]\n"
    "v_permlane32_swap_b32 v80, v84\n"
    "v_permlane32_swap_b32 v81, v85\n"
    "s_waitcnt lgkmcnt(8)\n"
    "v_mfma_f32_16x16x32_bf16 v[52:55], v[100:103], v[124:127], v[52:55]\n"
    "v_permlane32_swap_b32 v82, v86\n"
    "v_permlane32_swap_b32 v83, v87\n"
    "v_mfma_f32_16x16x32_bf16 v[56:59], v[104:107], v[64:67], 0\n"
    "v_pk_add_f32 v[80:81], v[80:81], v[84:85]\n"
    "v_pk_add_f32 v[82:83], v[82:83], v[86:87]\n"
    "v_mfma_f32_16x16x32_bf16 v[56:59], v[108:111], v[68:71], v[56:59]\n"
    "v_cvt_pk_bf16_f32 v84, v80, v81\n"
    "v_cvt_pk_bf16_f32 v85, v82, v83\n"
    "v_mfma_f32_16x16x32_bf16 v[56:59], v[112:115], v[120:123], v[56:59]\n"
    "buffer_store_dwordx2 v[84:85], v15, s[36:39], s75 offen sc1\n"
    "v_cndmask_b32 v80, 0, v84, s[80:81]\n"
    "v_mfma_f32_16x16x32_bf16 v[56:59], v[116:119], v[124:127], v[56:59]\n"
    "v_cndmask_b32 v81, 0, v85, s[80:81]\n"
    "v_cndmask_b32 v82, v84, 0, s[80:81]\n"
    "v_cndmask_b32 v83, v85, 0, s[80:81]\n"
    "s_waitcnt lgkmcnt(6)\n"
    "v_mfma_f32_16x16x32_bf16 v[48:51], v[72:75], v[64:67], 0\n"
    "ds_read_b128 v[88:91], v9\n"
    "ds_read_b128 v[92:95], v9 offset:32768\n"
    "s_waitcnt lgkmcnt(6)\n"
    "v_mfma_f32_16x16x32_bf16 v[48:51], v[76:79], v[68:71], v[48:51]\n"
    "ds_read_b128 v[96:99], v9 offset:1024\n"
    "ds_read_b128 v[100:103], v9 offset:33792\n"
    "s_waitcnt lgkmcnt(6)\n"
    "v_mfma_f32_16x16x32_bf16 v[48:51], v[32:35], v[120:123], v[48:51]\n"
    "v_max3_f32 v60, v52, v53, v54\n"
    "v_max3_f32 v60, v60, v55, v55\n"
    "s_waitcnt lgkmcnt(4)\n"
    "v_mfma_f32_16x16x32_bf16 v[48:51], v[36:39], v[124:127], v[48:51]\n"
    "v_max3_f32 v61, v56, v57, v58\n"
    "v_max3_f32 v60, v60, v61, v59\n"
    "ds_read_b128 v[104:107], v10\n"
    "ds_read_b128 v[108:111], v10 offset:32768\n"
    "ds_read_b128 v[112:115], v10 offset:1024\n"
    "ds_read_b128 v[116:119], v10 offset:33792\n"
    "ds_read_b128 v[32:35], v9 offset:2048\n"
    "ds_read_b128 v[36:39], v9 offset:34816\n"
    "ds_read_b128 v[40:43], v9 offset:3072\n"
    "ds_read_b128 v[44:47], v9 offset:35840\n"
    "v_max3_f32 v60, v60, v48, v49\n"
    "v_max3_f32 v61, v60, v50, v51\n"
    "v_max3_f32 v60, v60, v50, v51\n"
    "ds_read_b128 v[64:67], v10 offset:2048\n"
    "ds_read_b128 v[68:71], v10 offset:34816\n"
    "v_permlane16_swap_b32 v60, v61\n"
    "v_max_f32 v62, v60, v61\n"
    "v_max_f32 v60, v60, v61\n"
    "ds_read_b128 v[72:75], v10 offset:3072\n"
    "ds_read_b128 v[76:79], v10 offset:35840\n"
    "v_permlane32_swap_b32 v60, v62\n"
    "v_max_f32 v60, v60, v62\n"
    "v_mul_f32 v62, s78, v60\n"
    "v_pk_fma_f32 v[52:53], v[52:53], s[78:79], v[62:63] op_sel_hi:[1,0,0] neg_lo:[0,0,1] neg_hi:[0,0,1]\n"
    "v_pk_fma_f32 v[54:55], v[54:55], s[78:79], v[62:63] op_sel_hi:[1,0,0] neg_lo:[0,0,1] neg_hi:[0,0,1]\n"
    "v_pk_fma_f32 v[56:57], v[56:57], s[78:79], v[62:63] op_sel_hi:[1,0,0] neg_lo:[0,0,1] neg_hi:[0,0,1]\n"
    "v_pk_fma_f32 v[58:59], v[58:59], s[78:79], v[62:63] op_sel_hi:[1,0,0] neg_lo:[0,0,1] neg_hi:[0,0,1]\n"
    "v_pk_fma_f32 v[48:49], v[48:49], s[78:79], v[62:63] op_sel_hi:[1,0,0] neg_lo:[0,0,1] neg_hi:[0,0,1]\n"
    "v_pk_fma_f32 v[50:51], v[50:51], s[78:79], v[62:63] op_sel_hi:[1,0,0] neg_lo:[0,0,1] neg_hi:[0,0,1]\n"
    "v_exp_f32 v52, v52\n"
    "v_exp_f32 v53, v53\n"
    "v_exp_f32 v54, v54\n"
    "v_exp_f32 v55, v55\n"
    "v_exp_f32 v56, v56\n"
    "v_cvt_pk_bf16_f32 v52, v52, v53\n"
    "v_exp_f32 v57, v57\n"
    "v_cvt_pk_bf16_f32 v53, v54, v55\n"
    "v_exp_f32 v58, v58\n"
    "v_exp_f32 v59, v59\n"
    "v_exp_f32 v48, v48\n"
    "v_cvt_pk_bf16_f32 v54, v56, v57\n"
    "v_exp_f32 v49, v49\n"
    "v_cvt_pk_bf16_f32 v55, v58, v59\n"
    "v_exp_f32 v50, v50\n"
    "v_exp_f32 v51, v51\n"
    "v_mfma_f32_16x16x32_bf16 v[120:123], v[20:23], v[52:55], 0\n"
    "v_cvt_pk_bf16_f32 v48, v48, v49\n"
    "v_cvt_pk_bf16_f32 v49, v50, v51\n"
    "v_mfma_f32_16x16x32_bf16 v[56:59], v[24:27], v[52:55], 0\n"
    "s_nop 0\n"
    "v_mfma_f32_16x16x16_bf16 v[120:123], v[18:19], v[48:49], v[120:123]\n"
    "v_mfma_f32_16x16x32_bf16 v[60:63], v[28:31], v[52:55], 0\n"
    "v_mfma_f32_16x16x16_bf16 v[56:59], v[80:81], v[48:49], v[56:59]\n"
    "v_mfma_f32_16x16x16_bf16 v[60:63], v[82:83], v[48:49], v[60:63]\n"
    "v_mov_b64 v[48:49], 0\n"
    "v_mov_b64 v[50:51], 0\n"
    "v_mov_b64 v[52:53], 0\n"
    "v_mov_b64 v[54:55], 0\n"
    "s_nop 0\n"
    "v_rcp_f32 v124, v120\n"
    "v_mov_b64 v[80:81], 0\n"
    "v_mov_b64 v[82:83], 0\n"
    "v_mov_b64 v[84:85], 0\n"
    "v_pk_mul_f32 v[56:57], v[56:57], v[124:125] op_sel_hi:[1,0]\n"
    "v_pk_mul_f32 v[58:59], v[58:59], v[124:125] op_sel_hi:[1,0]\n"
    "v_pk_mul_f32 v[60:61], v[60:61], v[124:125] op_sel_hi:[1,0]\n"
    "v_pk_mul_f32 v[62:63], v[62:63], v[124:125] op_sel_hi:[1,0]\n"
    "v_cvt_pk_bf16_f32 v120, v56, v57\n"
    "v_cvt_pk_bf16_f32 v121, v58, v59\n"
    "v_cvt_pk_bf16_f32 v122, v60, v61\n"
    "v_cvt_pk_bf16_f32 v123, v62, v63\n"
    "ds_write_b128 v8, v[120:123]\n"
    "v_mov_b64 v[86:87], 0\n"
    "v_mov_b64 v[24:25], 0\n"
    "v_mov_b64 v[26:27], 0\n"
    "v_mov_b64 v[28:29], 0\n"
    "s_waitcnt lgkmcnt(0)\n"
    "s_barrier\n"
    "ds_read_b128 v[120:123], v0 offset:12288\n"
    "ds_read_b128 v[124:127], v0 offset:13312\n"
    "v_mov_b64 v[30:31], 0\n"
    "v_mov_b64 v[56:57], 0\n"
    "v_mov_b64 v[58:59], 0\n"
    "v_mov_b64 v[60:61], 0\n"
    "v_mov_b64 v[62:63], 0\n"
    "s_waitcnt lgkmcnt(1)\n"
    "v_smfmac_f32_16x16x64_bf16 v[48:51], v[120:123], v[88:95], v1\n"
    "ds_read_b128 v[88:91], v9 offset:4096\n"
    "ds_read_b128 v[92:95], v9 offset:36864\n"
    "v_smfmac_f32_16x16x64_bf16 v[52:55], v[120:123], v[96:103], v1\n"
    "ds_read_b128 v[96:99], v9 offset:5120\n"
    "ds_read_b128 v[100:103], v9 offset:37888\n"
    "s_waitcnt lgkmcnt(4)\n"
    "v_smfmac_f32_16x16x64_bf16 v[48:51], v[124:127], v[104:111], v1\n"
    "ds_read_b128 v[104:107], v10 offset:4096\n"
    "ds_read_b128 v[108:111], v10 offset:36864\n"
    "v_smfmac_f32_16x16x64_bf16 v[52:55], v[124:127], v[112:119], v1\n"
    "ds_read_b128 v[112:115], v10 offset:5120\n"
    "ds_read_b128 v[116:119], v10 offset:37888\n"
    "v_smfmac_f32_16x16x64_bf16 v[80:83], v[120:123], v[32:39], v1\n"
    "ds_read_b128 v[32:35], v9 offset:6144\n"
    "ds_read_b128 v[36:39], v9 offset:38912\n"
    "v_smfmac_f32_16x16x64_bf16 v[84:87], v[120:123], v[40:47], v1\n"
    "ds_read_b128 v[40:43], v9 offset:7168\n"
    "ds_read_b128 v[44:47], v9 offset:39936\n"
    "v_smfmac_f32_16x16x64_bf16 v[80:83], v[124:127], v[64:71], v1\n"
    "ds_read_b128 v[64:67], v10 offset:6144\n"
    "ds_read_b128 v[68:71], v10 offset:38912\n"
    "v_permlane32_swap_b32 v48, v52\n"
    "v_smfmac_f32_16x16x64_bf16 v[84:87], v[124:127], v[72:79], v1\n"
    "ds_read_b128 v[72:75], v10 offset:7168\n"
    "ds_read_b128 v[76:79], v10 offset:39936\n"
    "v_permlane32_swap_b32 v49, v53\n"
    "v_permlane32_swap_b32 v50, v54\n"
    "v_permlane32_swap_b32 v51, v55\n"
    "v_pk_add_f32 v[48:49], v[48:49], v[52:53]\n"
    "v_pk_add_f32 v[50:51], v[50:51], v[54:55]\n"
    "v_cvt_pk_bf16_f32 v52, v48, v49\n"
    "v_cvt_pk_bf16_f32 v53, v50, v51\n"
    "s_waitcnt lgkmcnt(14)\n"
    "v_smfmac_f32_16x16x64_bf16 v[24:27], v[120:123], v[88:95], v1\n"
    "s_waitcnt lgkmcnt(12)\n"
    "v_smfmac_f32_16x16x64_bf16 v[28:31], v[120:123], v[96:103], v1\n"
    "buffer_store_dwordx2 v[52:53], v11, s[36:39], s66 offen sc1\n"
    "s_waitcnt lgkmcnt(10)\n"
    "v_smfmac_f32_16x16x64_bf16 v[24:27], v[124:127], v[104:111], v1\n"
    "v_permlane32_swap_b32 v80, v84\n"
    "s_waitcnt lgkmcnt(8)\n"
    "v_smfmac_f32_16x16x64_bf16 v[28:31], v[124:127], v[112:119], v1\n"
    "v_permlane32_swap_b32 v81, v85\n"
    "v_permlane32_swap_b32 v82, v86\n"
    "v_permlane32_swap_b32 v83, v87\n"
    "v_pk_add_f32 v[80:81], v[80:81], v[84:85]\n"
    "v_pk_add_f32 v[82:83], v[82:83], v[86:87]\n"
    "v_cvt_pk_bf16_f32 v84, v80, v81\n"
    "v_cvt_pk_bf16_f32 v85, v82, v83\n"
    "s_waitcnt lgkmcnt(6)\n"
    "v_smfmac_f32_16x16x64_bf16 v[56:59], v[120:123], v[32:39], v1\n"
    "s_waitcnt lgkmcnt(4)\n"
    "v_smfmac_f32_16x16x64_bf16 v[60:63], v[120:123], v[40:47], v1\n"
    "buffer_store_dwordx2 v[84:85], v11, s[36:39], s67 offen sc1\n"
    "s_waitcnt lgkmcnt(2)\n"
    "v_smfmac_f32_16x16x64_bf16 v[56:59], v[124:127], v[64:71], v1\n"
    "v_permlane32_swap_b32 v24, v28\n"
    "s_waitcnt lgkmcnt(0)\n"
    "v_smfmac_f32_16x16x64_bf16 v[60:63], v[124:127], v[72:79], v1\n"
    "v_permlane32_swap_b32 v25, v29\n"
    "v_permlane32_swap_b32 v26, v30\n"
    "v_permlane32_swap_b32 v27, v31\n"
    "v_pk_add_f32 v[24:25], v[24:25], v[28:29]\n"
    "v_pk_add_f32 v[26:27], v[26:27], v[30:31]\n"
    "v_cvt_pk_bf16_f32 v28, v24, v25\n"
    "v_cvt_pk_bf16_f32 v29, v26, v27\n"
    "buffer_store_dwordx2 v[28:29], v11, s[36:39], s68 offen sc1\n"
    "v_permlane32_swap_b32 v56, v60\n"
    "v_permlane32_swap_b32 v57, v61\n"
    "v_permlane32_swap_b32 v58, v62\n"
    "v_permlane32_swap_b32 v59, v63\n"
    "v_pk_add_f32 v[56:57], v[56:57], v[60:61]\n"
    "v_pk_add_f32 v[58:59], v[58:59], v[62:63]\n"
    "v_cvt_pk_bf16_f32 v60, v56, v57\n"
    "v_cvt_pk_bf16_f32 v61, v58, v59\n"
    "buffer_store_dwordx2 v[60:61], v11, s[36:39], s69 offen sc1\n"
    "s_cmp_eq_u32 s60, 63\n"
    "s_cbranch_scc1 L_exit_%=\n"
    "s_add_u32 s84, s60, 1\n"
    "s_add_u32 s72, s72, 8192\n"
    "s_add_u32 s73, s73, 8192\n"
    "v_mov_b64 v[64:65], 0\n"
    "v_mov_b64 v[66:67], 0\n"
    "v_mov_b64 v[68:69], 0\n"
    "v_mov_b64 v[70:71], 0\n"
    "v_mov_b64 v[72:73], 0\n"
    "v_mov_b64 v[74:75], 0\n"
    "v_mov_b64 v[76:77], 0\n"
    "v_mov_b64 v[78:79], 0\n"
    "v_mov_b64 v[80:81], 0\n"
    "v_mov_b64 v[82:83], 0\n"
    "v_mov_b64 v[84:85], 0\n"
    "v_mov_b64 v[86:87], 0\n"
    "s_add_u32 s74, s74, 2048\n"
    "s_add_u32 s75, s75, 2048\n"
    "s_mov_b32 s82, 196611\n"
    "s_mov_b32 s83, 196611\n"
    "v_cndmask_b32 v104, 0, v17, s[82:83]\n"
    "s_mov_b32 s82, 786444\n"
    "s_mov_b32 s83, 786444\n"
    "v_cndmask_b32 v105, 0, v17, s[82:83]\n"
    "s_mov_b32 s82, 3145776\n"
    "s_mov_b32 s83, 3145776\n"
    "v_cndmask_b32 v106, 0, v17, s[82:83]\n"
    "s_mov_b32 s82, 12583104\n"
    "s_mov_b32 s83, 12583104\n"
    "v_cndmask_b32 v107, 0, v17, s[82:83]\n"
    "s_nop 15\n"
    "s_nop 15\n"
    "s_nop 15\n"
    "s_nop 15\n"
    "s_nop 15\n"
    "s_nop 15\n"
    "s_nop 3\n"
    "buffer_load_dwordx4 v[96:99], v12, s[36:39], s64 offen sc1\n"
    "buffer_load_dwordx4 v[100:103], v12, s[36:39], s64 offen offset:1024 sc1\n"
    "s_nop 15\n"
    "s_cmp_ge_i32 s62, s84\n"
    "s_cbranch_scc0 L_kvw1_%=\n"
    "L_kvok1_%=:\n"
    "buffer_load_dwordx4 v14, s[36:39], s72 offen offset:0 sc1 lds\n"
    "buffer_load_dwordx4 v14, s[36:39], s72 offen offset:1024 sc1 lds\n"
    "buffer_load_dwordx4 v[24:27], v14, s[36:39], s73 offen sc1\n"
    "buffer_load_dwordx4 v[28:31], v14, s[36:39], s73 offen offset:1024 sc1\n"
    "buffer_load_dword v112, off, s[36:39], s76 sc1\n"
    "buffer_load_dword v113, off, s[36:39], s77 sc1\n"
    "s_waitcnt vmcnt(7)\n"
    "L_chkA_%=:\n"
    "v_mfma_f32_16x16x32_bf16 v[40:43], v[96:99], v[104:107], 0\n"
    "s_waitcnt vmcnt(6)\n"
    "v_mfma_f32_16x16x32_bf16 v[40:43], v[100:103], v[104:107], v[40:43]\n"
    "s_nop 7\n"
    "v_pk_add_f32 v[40:41], v[40:41], v[42:43]\n"
    "v_add_f32 v40, v40, v41\n"
    "v_cmp_u_f32 vcc, v40, v40\n"
    "s_nop 0\n"
    "v_mov_b32_dpp v41, v40 row_shl:4 row_mask:0xf bank_mask:0xf\n"
    "s_cbranch_vccnz L_slowA_%=\n"
    "L_okA_%=:\n"
    "v_cvt_pk_bf16_f32 v40, v40, v41\n"
    "buffer_store_dword v40, v13, s[36:39], s70 offen sc1\n"
    "v_mov_b64 v[120:121], -1\n"
    "v_mov_b64 v[122:123], -1\n"
    "s_add_u32 s60, s60, 1\n"
    "s_mov_b32 s61, s70\n"
    "s_add_u32 s70, s70, 16384\n"
    "s_add_u32 s82, s89, 2146304\n"
    "s_cmp_eq_u32 s70, s82\n"
    "s_cselect_b32 s83, 49152, 0\n"
    "s_sub_u32 s70, s70, s83\n"
    "s_xor_b32 s66, s66, 524288\n"
    "s_xor_b32 s67, s67, 524288\n"
    "s_xor_b32 s68, s68, 524288\n"
    "s_xor_b32 s69, s69, 524288\n"
    "s_barrier\n"
    "buffer_store_dwordx4 v[120:123], v12, s[36:39], s64 offen sc1\n"
    "buffer_store_dwordx4 v[120:123], v12, s[36:39], s64 offen offset:1024 sc1\n"
    "buffer_store_dword v120, v13, s[36:39], s71 offen sc1\n"
    "s_nop 15\n"
    "s_nop 15\n"
    "s_nop 15\n"
    "s_nop 15\n"
    "s_nop 15\n"
    "s_nop 15\n"
    "s_nop 15\n"
    "s_nop 7\n"
    "buffer_load_dwordx4 v[32:35], v14, s[36:39], s61 offen sc1\n"
    "buffer_load_dwordx4 v[36:39], v14, s[36:39], s61 offen offset:1024 sc1\n"
    "s_xor_b32 s64, s64, 524288\n"
    "s_mov_b32 s71, s61\n"
    "s_waitcnt vmcnt(0)\n"
    "v_min_i32 v112, v112, v113\n"
    "L_chkB2_%=:\n"
    "v_max3_u32 v40, v32, v33, v34\n"
    "v_max3_u32 v41, v35, v36, v37\n"
    "v_readfirstlane_b32 s62, v112\n"
    "v_max3_u32 v40, v40, v38, v39\n"
    "v_max_u32 v40, v40, v41\n"
    "v_cmp_eq_u32 vcc, -1, v40\n"
    "s_cbranch_vccz L_top_%=\n"
    "s_branch L_retryB_%=\n"
    "L_slowA_%=:\n"
    "v_max3_u32 v44, v96, v97, v98\n"
    "v_max3_u32 v45, v99, v100, v101\n"
    "v_max3_u32 v44, v44, v102, v103\n"
    "v_max_u32 v44, v44, v45\n"
    "v_cmp_eq_u32 vcc, -1, v44\n"
    "s_nop 1\n"
    "s_cbranch_vccz L_okA_%=\n"
    "s_add_u32 s63, s63, 1\n"
    "s_cmp_gt_u32 s63, 2097152\n"
    "s_cbranch_scc1 L_okA_%=\n"
    "buffer_load_dwordx4 v[96:99], v12, s[36:39], s64 offen offset:0 sc1\n"
    "buffer_load_dwordx4 v[100:103], v12, s[36:39], s64 offen offset:1024 sc1\n"
    "s_waitcnt vmcnt(0)\n"
    "s_branch L_chkA_%=\n"
    "L_retryB_%=:\n"
    "s_add_u32 s63, s63, 1\n"
    "s_cmp_gt_u32 s63, 2097152\n"
    "s_cbranch_scc1 L_top_%=\n"
    "buffer_load_dwordx4 v[32:35], v14, s[36:39], s61 offen offset:0 sc1\n"
    "buffer_load_dwordx4 v[36:39], v14, s[36:39], s61 offen offset:1024 sc1\n"
    "s_waitcnt vmcnt(0)\n"
    "s_branch L_chkB2_%=\n"
    "L_kvw0_%=:\n"
    "buffer_load_dword v114, off, s[36:39], s76 sc1\n"
    "buffer_load_dword v115, off, s[36:39], s77 sc1\n"
    "s_waitcnt vmcnt(0)\n"
    "v_min_i32 v114, v114, v115\n"
    "s_nop 1\n"
    "v_readfirstlane_b32 s62, v114\n"
    "s_nop 1\n"
    "s_cmp_ge_i32 s62, s84\n"
    "s_cbranch_scc1 L_kvok0_%=\n"
    "s_add_u32 s63, s63, 1\n"
    "s_cmp_gt_u32 s63, 2097152\n"
    "s_cbranch_scc1 L_kvok0_%=\n"
    "s_sleep 2\n"
    "s_branch L_kvw0_%=\n"
    "L_kvw1_%=:\n"
    "buffer_load_dword v114, off, s[36:39], s76 sc1\n"
    "buffer_load_dword v115, off, s[36:39], s77 sc1\n"
    "s_waitcnt vmcnt(0)\n"
    "v_min_i32 v114, v114, v115\n"
    "s_nop 1\n"
    "v_readfirstlane_b32 s62, v114\n"
    "s_nop 1\n"
    "s_cmp_ge_i32 s62, s84\n"
    "s_cbranch_scc1 L_kvok1_%=\n"
    "s_add_u32 s63, s63, 1\n"
    "s_cmp_gt_u32 s63, 2097152\n"
    "s_cbranch_scc1 L_kvok1_%=\n"
    "s_sleep 2\n"
    "s_branch L_kvw1_%=\n"
    "L_exit_%=:\n"
    "s_waitcnt vmcnt(0) lgkmcnt(0)\n"
    "s_barrier\n"
    "v_mov_b32 v17, 63\n"
    "s_nop 0\n"
    "buffer_store_dword v17, v16, s[36:39], 0 offen sc1\n"
    "s_waitcnt vmcnt(0)\n"
      : "+{v[0:31]}"(c), "+{v[32:47]}"(d)
      : "{s[36:51]}"(sa), "{s[52:59]}"(sb)
      : "memory", "vcc", "scc",
      "s60", "s61", "s62", "s63", "s64", "s65", "s66", "s67", "s68", "s69", "s70", "s71", "s72", "s73", "s74", "s75",
      "s76", "s77", "s78", "s79", "s80", "s81", "s82", "s83", "s84", "s85", "s86", "s87", "s88", "s89", "s90", "s91",
      "s92", "s93", "s94", "s95", "v48", "v49", "v50", "v51", "v52", "v53", "v54", "v55", "v56", "v57", "v58", "v59",
      "v60", "v61", "v62", "v63", "v64", "v65", "v66", "v67", "v68", "v69", "v70", "v71", "v72", "v73", "v74", "v75",
      "v76", "v77", "v78", "v79", "v80", "v81", "v82", "v83", "v84", "v85", "v86", "v87", "v88", "v89", "v90", "v91",
      "v92", "v93", "v94", "v95", "v96", "v97", "v98", "v99", "v100", "v101", "v102", "v103", "v104", "v105", "v106", "v107",
      "v108", "v109", "v110", "v111", "v112", "v113", "v114", "v115", "v116", "v117", "v118", "v119", "v120", "v121", "v122", "v123",
      "v124", "v125", "v126", "v127", "v128", "v129", "v130", "v131", "v132", "v133", "v134", "v135", "v136", "v137", "v138", "v139",
      "v140", "v141", "v142", "v143", "v144", "v145", "v146", "v147", "v148", "v149", "v150", "v151", "v152", "v153", "v154", "v155",
      "v156", "v157", "v158", "v159", "v160", "v161", "v162", "v163", "v164", "v165", "v166", "v167", "v168", "v169", "v170", "v171",
      "v172", "v173", "v174", "v175", "v176", "v177", "v178", "v179", "v180", "v181", "v182", "v183", "v184", "v185", "v186", "v187",
      "v188", "v189", "v190", "v191", "v192", "v193", "v194", "v195", "v196", "v197", "v198", "v199", "v200", "v201", "v202", "v203",
      "v204", "v205", "v206", "v207", "v208", "v209", "v210", "v211", "v212", "v213", "v214", "v215", "v216", "v217", "v218", "v219",
      "v220", "v221", "v222", "v223", "v224", "v225", "v226", "v227", "v228", "v229", "v230", "v231", "v232", "v233", "v234", "v235",
      "v236", "v237", "v238", "v239", "v240", "v241", "v242", "v243", "v244", "v245", "v246", "v247", "v248", "v249", "v250", "v251",
      "v252", "v253", "v254", "v255", "a0", "a1", "a2", "a3", "a4", "a5", "a6", "a7", "a8", "a9", "a10", "a11",
      "a12", "a13", "a14", "a15", "a16", "a17", "a18", "a19", "a20", "a21", "a22", "a23", "a24", "a25", "a26", "a27",
      "a28", "a29", "a30", "a31", "a32", "a33", "a34", "a35", "a36", "a37", "a38", "a39", "a40", "a41", "a42", "a43",
      "a44", "a45", "a46", "a47", "a48", "a49", "a50", "a51", "a52", "a53", "a54", "a55", "a56", "a57", "a58", "a59",
      "a60", "a61", "a62", "a63", "a64", "a65", "a66", "a67", "a68", "a69", "a70", "a71", "a72", "a73", "a74", "a75",
      "a76", "a77", "a78", "a79", "a80", "a81", "a82", "a83", "a84", "a85", "a86", "a87", "a88", "a89", "a90", "a91",
      "a92", "a93", "a94", "a95", "a96", "a97", "a98", "a99", "a100", "a101", "a102", "a103", "a104", "a105", "a106", "a107",
      "a108", "a109", "a110", "a111", "a112", "a113", "a114", "a115", "a116", "a117", "a118", "a119", "a120", "a121", "a122", "a123",
      "a124", "a125", "a126", "a127", "a128", "a129", "a130", "a131", "a132", "a133", "a134", "a135", "a136", "a137", "a138", "a139",
      "a140", "a141", "a142", "a143", "a144", "a145", "a146", "a147", "a148", "a149", "a150", "a151", "a152", "a153", "a154", "a155",
      "a156", "a157", "a158", "a159", "a160", "a161", "a162", "a163", "a164", "a165", "a166", "a167", "a168", "a169", "a170", "a171",
      "a172", "a173", "a174", "a175", "a176", "a177", "a178", "a179", "a180", "a181", "a182", "a183", "a184", "a185", "a186", "a187",
      "a188", "a189", "a190", "a191", "a192", "a193", "a194", "a195", "a196", "a197", "a198", "a199", "a200", "a201", "a202", "a203",
      "a204", "a205", "a206", "a207", "a208", "a209", "a210", "a211", "a212", "a213", "a214", "a215", "a216", "a217", "a218", "a219",
      "a220", "a221", "a222", "a223", "a224", "a225", "a226", "a227", "a228", "a229", "a230", "a231", "a232", "a233", "a234", "a235",
      "a236", "a237", "a238", "a239", "a240", "a241", "a242", "a243", "a244", "a245", "a246", "a247", "a248", "a249", "a250", "a251",
      "a252", "a253", "a254", "a255"
  );
}

constexpr unsigned XB0 = 131072, XB1 = 0;
DI void load_w_frags(v4u (&wf)[2][16], const void* W, int h, int w, int lane, char* L, unsigned ldsb) {
  const int gb = lane >> 4, qq = (lane >> 2) & 3, pp = lane & 3;
  const v4u srd = mksrd(W, 512 * 4096 * 2);
  unsigned vo[2];
#pragma unroll
  for (int b = 0; b < 2; b++) {
    unsigned f = gb | (b << 2);
    vo[b] = gb * 8192 + (((((lane & 15) >> 1) ^ f) << 1) | (lane & 1)) * 16;
  }
#pragma unroll
  for (int cc = 0; cc < 4; cc++)
#pragma unroll
    for (int i = 0; i < 8; i++) {
      unsigned row = 128 * cc + 32 * w + 4 * i;
      dma16(srd, vo[(i >> 1) & 1], row * 8192 + h * 256, ldsb + row * 256);
    }
  wait_vm<0>();
  bar();
  const unsigned f = qq | ((gb & 1) << 2);
  const unsigned b = (8 * gb + qq) * 256 + pp * 8;
#pragma unroll
  for (int tl = 0; tl < 2; tl++) {
    const unsigned bt = b + (((2 * w + tl) ^ f) * 32);
#pragma unroll
    for (int ks = 0; ks < 16; ks++) {
      v2u a = trd(L, bt + (32 * ks) * 256), c = trd(L, bt + (32 * ks + 4) * 256);
      wf[tl][ks] = v4u{a.x, a.y, c.x, c.y};
    }
  }
  bar();
}
DI void issue_x(v4u srdx, int s, int w, int lane, unsigned ldsb, unsigned xb) {
#pragma unroll
  for (int i = 0; i < 8; i++) {
    unsigned row = 8 * w + i;
    dma16(srdx, (lane ^ (row & 15)) * 16, (s * 32 + row) * 1024, ldsb + xb + row * 1024);
  }
}
DI v4u read_x(const char* L, unsigned xb, int tt, int ks, int lane) {
  const int r = lane & 15, g = lane >> 4;
  return *(const v4u*)(L + xb + (16 * tt + r) * 1024 + (((4 * ks + g) ^ r) * 16));
}

template <int TYPE>
DI void kv_role(int h, char* L, unsigned ldsb, rsrc_t ws, const void* x, const void* W, unsigned fsw) {
  const int t = threadIdx.x, lane = t & 63;
  const int w = __builtin_amdgcn_readfirstlane(t >> 6);
  const v4u srdx = mksrd(x, 2048 * 512 * 2);
  TRDECL; TRR(12);
  issue_x(srdx, 0, w, lane, ldsb, XB0);
  v4u wf[2][16];
  load_w_frags(wf, W, h, w, lane, L, ldsb);
  TRR(13);
  unsigned spins = 0;
  int mknown = -1;
  for (int s = 0; s < NSEG; s++) {
    TRR(0);
    const unsigned cur = (s & 1) ? XB1 : XB0;
    if (s + 1 < NSEG) issue_x(srdx, s + 1, w, lane, ldsb, (s & 1) ? XB0 : XB1);
    f32x4 acc[2][2] = {};
#pragma unroll
    for (int ks = 0; ks < 16; ks++) {
      v4u x0 = read_x(L, cur, 0, ks, lane), x1 = read_x(L, cur, 1, ks, lane);
#pragma unroll
      for (int tl = 0; tl < 2; tl++) {
        if (TYPE == 0) { acc[tl][0] = mfma16(wf[tl][ks], x0, acc[tl][0]); acc[tl][1] = mfma16(wf[tl][ks], x1, acc[tl][1]); }
        else { acc[tl][0] = mfma16(x0, wf[tl][ks], acc[tl][0]); acc[tl][1] = mfma16(x1, wf[tl][ks], acc[tl][1]); }
      }
    }
    if (TYPE == 0) {
#pragma unroll
      for (int tt = 0; tt < 2; tt++) {
        v4u o = {pk(acc[0][tt][0], acc[0][tt][1]), pk(acc[0][tt][2], acc[0][tt][3]), pk(acc[1][tt][0], acc[1][tt][1]),
                 pk(acc[1][tt][2], acc[1][tt][3])};
        st16(ws, OFF_KX + (h * NSEG + s) * 8192 + (tt * 4 + w) * 1024 + lane * 16, o);
      }
    } else {
#pragma unroll
      for (int tl = 0; tl < 2; tl++) {
        v4u o = {pk(acc[tl][0][0], acc[tl][0][1]), pk(acc[tl][0][2], acc[tl][0][3]), pk(acc[tl][1][0], acc[tl][1][1]),
                 pk(acc[tl][1][2], acc[tl][1][3])};
        st16(ws, OFF_VX + (h * NSEG + s) * 8192 + (2 * w + tl) * 1024 + lane * 16, o);
      }
    }
    if (s < KV_LAZY || s == NSEG - 1) {
      wait_vm<0>();
      bar();
      if (t == 0) st4(ws, (FLG_KV ^ fsw) + (TYPE * 32 + h) * 128, s);
    } else {
      wait_vm<2>();
      bar();
      if (t == 0) st4(ws, (FLG_KV ^ fsw) + (TYPE * 32 + h) * 128, s - 1);
    }
    TRR(1); TRFLUSH(s);
  }
}

constexpr unsigned QA_QX = 32768, QA_KT = 40960;
DI void qa_role(int par, int h, char* L, unsigned ldsb, rsrc_t ws, const void* x, const void* Wq, unsigned fsw) {
  const int t = threadIdx.x, lane = t & 63;
  const int w = __builtin_amdgcn_readfirstlane(t >> 6);
  const int mh = w & 1, eh = w >> 1, g = lane >> 4, c = lane & 15;
  const unsigned kxw = (16 * (lane >> 5) + (lane & 15)) * 16 + ((lane >> 4) & 1) * 8;
  const unsigned kxr = (4 * g + ((lane >> 2) & 3)) * 16 + (lane & 1) * 8;
  const v4u srdx = mksrd(x, 2048 * 512 * 2);
  unsigned spins = 0;
  TRDECL; TRR(12);
  issue_x(srdx, par, w, lane, ldsb, XB0);
  v4u wf[2][16];
  load_w_frags(wf, Wq, h, w, lane, L, ldsb);
  TRR(13);
  int kvknown = -1;
  for (int s = par; s < NSEG; s += 2) {
    TRR(0);
    const unsigned cur = (s & 2) ? XB1 : XB0;
    v4u kx[2][4], vx[4];
    if (kvknown < s) {
      int a = wait_flag(ws, (FLG_KV ^ fsw) + h * 128, s, spins);
      int b = wait_flag(ws, (FLG_KV ^ fsw) + (32 + h) * 128, s, spins);
      kvknown = min(a, b);
    }
#pragma unroll
    for (int i = 0; i < 8; i++) kx[i >> 2][i & 3] = ld16(ws, OFF_KX + (h * NSEG + s) * 8192 + i * 1024 + lane * 16);
#pragma unroll
    for (int i = 0; i < 4; i++) vx[i] = ld16(ws, OFF_VX + (h * NSEG + s) * 8192 + (4 * eh + i) * 1024 + lane * 16);
    if (s + 2 < NSEG) issue_x(srdx, s + 2, w, lane, ldsb, (s & 2) ? XB0 : XB1);
    {
      f32x4 acc[2][2] = {};
#pragma unroll
      for (int ks = 0; ks < 16; ks++) {
        v4u x0 = read_x(L, cur, 0, ks, lane), x1 = read_x(L, cur, 1, ks, lane);
#pragma unroll
        for (int tl = 0; tl < 2; tl++) {
          acc[tl][0] = mfma16(wf[tl][ks], x0, acc[tl][0]);
          acc[tl][1] = mfma16(wf[tl][ks], x1, acc[tl][1]);
        }
      }
#pragma unroll
      for (int tt = 0; tt < 2; tt++) {
        v4u o = {pk(acc[0][tt][0], acc[0][tt][1]), pk(acc[0][tt][2], acc[0][tt][3]), pk(acc[1][tt][0], acc[1][tt][1]),
                 pk(acc[1][tt][2], acc[1][tt][3])};
        *(v4u*)(L + QA_QX + (tt * 4 + w) * 1024 + lane * 16) = o;
      }
    }
    bar();
    TRR(5);
    v4u qf[4];
#pragma unroll
    for (int kk = 0; kk < 4; kk++) qf[kk] = *(const v4u*)(L + QA_QX + (mh * 4 + kk) * 1024 + lane * 16);
    const int tok = 16 * mh + c;
    float ex[8], mxx, sumx;
    {
      f32x4 s0 = {0, 0, 0, 0}, s1 = s0;
#pragma unroll
      for (int kk = 0; kk < 4; kk++) {
        s0 = mfma16(kx[0][kk], qf[kk], s0);
        s1 = mfma16(kx[1][kk], qf[kk], s1);
      }
#pragma unroll
      for (int i = 0; i < 4; i++) {
        ex[i] = (4 * g + i <= tok) ? rbf(rbf(s0[i]) * SCALE) : NEG_MASK;
        ex[4 + i] = (16 + 4 * g + i <= tok) ? rbf(rbf(s1[i]) * SCALE) : NEG_MASK;
      }
      TRR(6);
      mxx = fmaxf(fmaxf(fmaxf(ex[0], ex[1]), fmaxf(ex[2], ex[3])), fmaxf(fmaxf(ex[4], ex[5]), fmaxf(ex[6], ex[7])));
      mxx = xmax32(xmax16(mxx));
#pragma unroll
      for (int i = 0; i < 8; i++) ex[i] = __builtin_amdgcn_exp2f((ex[i] - mxx) * LOG2E);
      sumx = ((ex[0] + ex[1]) + (ex[2] + ex[3])) + ((ex[4] + ex[5]) + (ex[6] + ex[7]));
    }
    TRR(1);
    const bool direct = s >= NSEG - NDIR;
    const unsigned okm = OFF_KM + (h * NSEG + s) * 2048 + lane * 8, ovm = OFF_VM + (h * NSEG + s) * 2048 + 2 * eh * 512 + lane * 8;
    v2u kp[4], vm[2];
    if (!direct) {
      wait_flag(ws, (FLG_M ^ fsw) + h * 128, s, spins);
#pragma unroll
      for (int i = 0; i < 4; i++) kp[i] = ld8(ws, okm + i * 512);
#pragma unroll
      for (int i = 0; i < 2; i++) vm[i] = ld8(ws, ovm + i * 512);
    } else {
      while (true) {
        asm volatile("" ::: "memory");
        const int fl = (int)ld4(ws, (FLG_M ^ fsw) + h * 128);
#pragma unroll
        for (int i = 0; i < 4; i++) kp[i] = ld8(ws, okm + i * 512);
#pragma unroll
        for (int i = 0; i < 2; i++) vm[i] = ld8(ws, ovm + i * 512);
        const unsigned m = max(max(max(kp[0].x, kp[0].y), max(kp[1].x, kp[1].y)), max(max(kp[2].x, kp[2].y), max(kp[3].x, kp[3].y)));
        if (!__any(m == POIS)) break;
        if (__builtin_amdgcn_readfirstlane(fl) >= s || ++spins > SPIN_LIMIT) {
          asm volatile("" ::: "memory");
#pragma unroll
          for (int i = 0; i < 4; i++) kp[i] = ld8(ws, okm + i * 512);
          break;
        }
        __builtin_amdgcn_s_sleep(1);
      }
    }
    TRR(2);
    v4u km[4];
    {
      char* KT = L + QA_KT + w * 2048;
#pragma unroll
      for (int i = 0; i < 4; i++) *(v2u*)(KT + kxw + i * 512) = kp[i];
      asm volatile("s_waitcnt lgkmcnt(0)" ::: "memory");
#pragma unroll
      for (int kk = 0; kk < 4; kk++) {
        v2u a = trd(KT, kxr + kk * 512), b = trd(KT, kxr + kk * 512 + 256);
        km[kk] = v4u{a.x, a.y, b.x, b.y};
      }
    }
    f32x4 sm = {0, 0, 0, 0};
#pragma unroll
    for (int kk = 0; kk < 4; kk++) sm = mfma16(km[kk], qf[kk], sm);
    float p[4];
#pragma unroll
    for (int i = 0; i < 4; i++) p[i] = rbf(rbf(sm[i]) * SCALE);
    const float mx = fmaxf(mxx, xmax32(xmax16(fmaxf(fmaxf(p[0], p[1]), fmaxf(p[2], p[3])))));
#pragma unroll
    for (int i = 0; i < 4; i++) p[i] = __builtin_amdgcn_exp2f((p[i] - mx) * LOG2E);
    const float fx = __builtin_amdgcn_exp2f((mxx - mx) * LOG2E);
    const bool lo = lane < 32;
    float sum = (lo ? (p[0] + p[1]) + (p[2] + p[3]) : 0.f) + fx * sumx;
    sum = xsum32(xsum16(sum));
    const float rs = __builtin_amdgcn_rcpf(sum), rx = fx * rs;
    unsigned pm0 = pk(p[0] * rs, p[1] * rs), pm1 = pk(p[2] * rs, p[3] * rs);
    v2u pa = {lo ? pm0 : 0u, lo ? pm1 : 0u}, pb = {lo ? 0u : pm0, lo ? 0u : pm1};
    v4u px = {pk(ex[0] * rx, ex[1] * rx), pk(ex[2] * rx, ex[3] * rx), pk(ex[4] * rx, ex[5] * rx), pk(ex[6] * rx, ex[7] * rx)};
    f32x4 o[4];
#pragma unroll
    for (int i = 0; i < 4; i++) o[i] = mfma16(vx[i], px, f32x4{0, 0, 0, 0});
    if (direct) {
      while (__any(max(max(vm[0].x, vm[0].y), max(vm[1].x, vm[1].y)) == POIS)) {
        asm volatile("" ::: "memory");
        const int fl = (int)ld4(ws, (FLG_M ^ fsw) + h * 128);
#pragma unroll
        for (int i = 0; i < 2; i++) vm[i] = ld8(ws, ovm + i * 512);
        if (!__any(max(max(vm[0].x, vm[0].y), max(vm[1].x, vm[1].y)) == POIS)) break;
        if (__builtin_amdgcn_readfirstlane(fl) >= s || ++spins > SPIN_LIMIT) {
          asm volatile("" ::: "memory");
#pragma unroll
          for (int i = 0; i < 2; i++) vm[i] = ld8(ws, ovm + i * 512);
          break;
        }
      }
    }
#pragma unroll
    for (int i = 0; i < 4; i++)
      o[i] = __builtin_amdgcn_mfma_f32_16x16x16bf16_1k(__builtin_bit_cast(s16x4, vm[i >> 1]), __builtin_bit_cast(s16x4, (i & 1) ? pb : pa), o[i], 0, 0, 0);
#pragma unroll
    for (int i = 0; i < 2; i++) {
      v4u u = {pk(o[2 * i][0], o[2 * i][1]), pk(o[2 * i][2], o[2 * i][3]), pk(o[2 * i + 1][0], o[2 * i + 1][1]),
               pk(o[2 * i + 1][2], o[2 * i + 1][3])};
      st16(ws, OFF_OX + (s * 32 + h) * 8192 + (mh * 4 + 2 * eh + i) * 1024 + lane * 16, u);
    }
    if (s < QA_LAZY) {
      wait_vm<2>();
      bar();
      if (t == 0) st4(ws, (FLG_O ^ fsw) + h * 128 + par * 4, s - 2);
    } else
    {
      wait_vm<0>();
      bar();
      if (t == 0) st4(ws, (FLG_O ^ fsw) + h * 128 + par * 4, s);
    }
    TRR(3); TRFLUSH(s);
  }
}

DI void wo_role(int par, int mh, int ng, char* L, unsigned ldsb, rsrc_t ws, const void* Wo, void* out, unsigned fsw) {
  const int t = threadIdx.x, lane = t & 63;
  const int w = __builtin_amdgcn_readfirstlane(t >> 6);
  const int g = lane >> 4, qq = (lane >> 2) & 3, pp = lane & 3;
  unsigned spins = 0;
  TRDECL; TRR(12);
  v4u wo[32][2];
  {
    const v4u srd = mksrd(Wo, 4096 * 512 * 2);
    const unsigned vo = (lane >> 2) * 1024 + (lane & 3) * 16;
    const unsigned lb = w * 32768;
#pragma unroll
    for (int rd = 0; rd < 2; rd++) {
#pragma unroll
      for (int i = 0; i < 32; i++)
        dma16(srd, vo, ((8 * w + 4 * rd) * 128 + 16 * i) * 1024 + ng * 64, ldsb + lb + i * 1024);
      wait_vm<0>();
#pragma unroll
      for (int hl = 0; hl < 4; hl++)
#pragma unroll
        for (int kk = 0; kk < 4; kk++)
#pragma unroll
          for (int ct = 0; ct < 2; ct++) {
            unsigned b = lb + (hl * 128 + 32 * kk + 4 * g + qq) * 64 + pp * 16 + ct * 8;
            v2u a = trd(L, b), c = trd(L, b + 16 * 64);
            wo[(rd * 4 + hl) * 4 + kk][ct] = v4u{a.x, a.y, c.x, c.y};
          }
      asm volatile("s_waitcnt lgkmcnt(0)" ::: "memory");
    }
  }
  bar();
  rsrc_t ro = mkrsrc(out, 2048 * 1024 * 2);
  int it = 0;
  TRR(13);
  for (int s = par; s < NSEG; s += 2, it ^= 1) {
    TRR(0);
    while (true) {
      asm volatile("" ::: "memory");
      int v = (int)ld4(ws, (FLG_O ^ fsw) + (8 * w + (lane & 7)) * 128 + par * 4);
      if (__all(v >= s)) break;
      if (++spins > SPIN_LIMIT) break;
      if (s < NSEG - LAST_N) __builtin_amdgcn_s_sleep(WO_LONG); else __builtin_amdgcn_s_sleep(WO_SLEEP);
    }
    TRR(1);
    f32x4 a0 = {0, 0, 0, 0}, a1 = a0;
    {
      v4u of[32];
#pragma unroll
      for (int ks = 0; ks < 32; ks++)
        of[ks] = ld16(ws, OFF_OX + (s * 32 + 8 * w + (ks >> 2)) * 8192 + (mh * 4 + (ks & 3)) * 1024 + lane * 16);
      __builtin_amdgcn_sched_barrier(0);
#pragma unroll
      for (int ks = 0; ks < 32; ks++) {
        a0 = mfma16(wo[ks][0], of[ks], a0);
        a1 = mfma16(wo[ks][1], of[ks], a1);
      }
      __builtin_amdgcn_sched_barrier(0);
    }
    char* R = L + it * 8192;
    *(f32x4*)(R + w * 2048 + lane * 16) = a0;
    *(f32x4*)(R + w * 2048 + 1024 + lane * 16) = a1;
    bar();
    if (w == 0) {
      f32x4 r0 = {0, 0, 0, 0}, r1 = r0;
#pragma unroll
      for (int i = 0; i < 4; i++) {
        r0 += *(const f32x4*)(R + i * 2048 + lane * 16);
        r1 += *(const f32x4*)(R + i * 2048 + 1024 + lane * 16);
      }
      v4u o = {pk(r0[0], r0[1]), pk(r0[2], r0[3]), pk(r1[0], r1[1]), pk(r1[2], r1[3])};
      stp16(ro, (s * 32 + 16 * mh + (lane & 15)) * 2048 + 1024 + ng * 64 + g * 16, o);
    }
    TRR(2); TRFLUSH(s);
  }
}

constexpr unsigned CV_KV = 0, CV_CH = 5200, CV_Y = 41600, CV_H = 83200, CV_HS = 4160;
DI void cv_role(int cv, char* L, rsrc_t ws, const void* q, const void* k, const void* v, const void* shk, const void* ssk,
                const void* ssd, void* out) {
  const int t = threadIdx.x, lane = t & 63;
  const int w = __builtin_amdgcn_readfirstlane(t >> 6);
  const int d0 = cv * 8;
  TRDECL; TRR(12);
  rsrc_t rq = mkrsrc(q, 2048 * 512 * 2), rk = mkrsrc(k, 2048 * 512 * 2), rv = mkrsrc(v, 2048 * 512 * 2);
  {
    rsrc_t rs = mkrsrc(shk, 512 * 4 * 2);
    v4u sh[4];
#pragma unroll
    for (int i = 0; i < 4; i++) sh[i] = ldp16(rs, d0 * 8 + i * 16);
    v4u kr[11], vr[8];
#pragma unroll
    for (int i = 0; i < 11; i++) {
      int s = 8 * t + i - 3;
      kr[i] = ldp16(rk, s < 0 ? 0x80000000u : (unsigned)(s * 1024 + d0 * 2));
    }
#pragma unroll
    for (int i = 0; i < 8; i++) vr[i] = ldp16(rv, (8 * t + i) * 1024 + d0 * 2);
    if (t < 40) *(v4u*)(L + CV_KV + (t / 5) * CV_CH + (t % 5) * 16) = v4u{0, 0, 0, 0};
#pragma unroll
    for (int ch = 0; ch < 8; ch++) {
      float tap[4];
      {
        unsigned u0 = sh[ch >> 1][(ch & 1) * 2], u1 = sh[ch >> 1][(ch & 1) * 2 + 1];
        tap[0] = bflo(u0); tap[1] = bfhi(u0); tap[2] = bflo(u1); tap[3] = bfhi(u1);
      }
      float r[8];
#pragma unroll
      for (int i = 0; i < 8; i++) {
        float a = 0.f;
#pragma unroll
        for (int tt = 0; tt < 4; tt++) {
          unsigned u = kr[i + tt][ch >> 1];
          float kk = (ch & 1) ? bfhi(u) : bflo(u);
          a = rbf(a + rbf(kk * tap[3 - tt]));
        }
        unsigned uv = vr[i][ch >> 1];
        r[i] = a * ((ch & 1) ? bfhi(uv) : bflo(uv));
      }
      v4u o = {pk(r[0], r[1]), pk(r[2], r[3]), pk(r[4], r[5]), pk(r[6], r[7])};
      *(v4u*)(L + CV_KV + ch * CV_CH + (1 + (t >> 2)) * 80 + (t & 3) * 16) = o;
    }
  }
  bar();
  TRR(13);
  const int r = lane & 31, kg = lane >> 5;
  for (int cc = 0; cc < 2; cc++) {
    const int ch = 2 * w + cc;
    char* H = L + CV_H + w * (4 * CV_HS);
    char* HL = L + CV_Y + ch * CV_CH;
    {
      rsrc_t rh = mkrsrc(ssk, 512 * 2048 * 2);
#pragma unroll
      for (int i = 0; i < 4; i++) *(v4u*)(HL + i * 1024 + lane * 16) = ldp16(rh, (d0 + ch) * 4096 + i * 1024 + lane * 16);
      asm volatile("s_waitcnt lgkmcnt(0)" ::: "memory");
      for (int u = lane; u < 4 * 520; u += 64) {
        const int sg = u / 520, m = (u % 520) * 4;
        unsigned e[4];
#pragma unroll
        for (int j = 0; j < 4; j++) {
          int n = 2048 - m - j + sg;
          unsigned val = *(const unsigned short*)(HL + (n & 2047) * 2);
          e[j] = (n >= 0 && n < 2048) ? val : 0u;
        }
        *(v2u*)(H + sg * CV_HS + m * 2) = v2u{e[0] | (e[1] << 16), e[2] | (e[3] << 16)};
      }
      asm volatile("s_waitcnt lgkmcnt(0)" ::: "memory");
    }
    const char* U = L + CV_KV + ch * CV_CH;
    const unsigned ha = (r & 3) * CV_HS + (2048 - 4 * (r >> 2) + 8 * kg) * 2;
    f32x16 y0 = {}, y1 = {};
#pragma unroll 2
    for (int dl = 0; dl < 32; dl++) {
      const int bi = max(r - dl + 1, 0);
#pragma unroll
      for (int ks = 0; ks < 2; ks++) {
        const char* pa = H + ha - dl * 64 + ks * 32;
        v2u a00 = *(const v2u*)(pa), a01 = *(const v2u*)(pa + 8);
        v2u a10 = *(const v2u*)(pa - 2048), a11 = *(const v2u*)(pa - 2048 + 8);
        v4u b0 = *(const v4u*)(U + bi * 80 + ks * 32 + kg * 16);
        v4u b1 = *(const v4u*)(U + (r + 33 - dl) * 80 + ks * 32 + kg * 16);
        bf16x8 A0 = __builtin_bit_cast(bf16x8, v4u{a00.x, a00.y, a01.x, a01.y});
        bf16x8 A1 = __builtin_bit_cast(bf16x8, v4u{a10.x, a10.y, a11.x, a11.y});
        y0 = __builtin_amdgcn_mfma_f32_32x32x16_bf16(A0, __builtin_bit_cast(bf16x8, b0), y0, 0, 0, 0);
        y1 = __builtin_amdgcn_mfma_f32_32x32x16_bf16(A0, __builtin_bit_cast(bf16x8, b1), y1, 0, 0, 0);
        y1 = __builtin_amdgcn_mfma_f32_32x32x16_bf16(A1, __builtin_bit_cast(bf16x8, b0), y1, 0, 0, 0);
      }
    }
    char* Y = L + CV_Y + ch * CV_CH;
#pragma unroll
    for (int n = 0; n < 4; n++) {
      *(v2u*)(Y + (1 + r) * 80 + (8 * n + 4 * kg) * 2) = v2u{pk(y0[4 * n], y0[4 * n + 1]), pk(y0[4 * n + 2], y0[4 * n + 3])};
      *(v2u*)(Y + (33 + r) * 80 + (8 * n + 4 * kg) * 2) = v2u{pk(y1[4 * n], y1[4 * n + 1]), pk(y1[4 * n + 2], y1[4 * n + 3])};
    }
    asm volatile("s_waitcnt lgkmcnt(0)" ::: "memory");
  }
  bar();
  TRR(0);
  {
    rsrc_t rd = mkrsrc(ssd, 512 * 2), ro = mkrsrc(out, 2048 * 1024 * 2);
    v4u dv = ldp16(rd, d0 * 2);
    v4u qr[8];
#pragma unroll
    for (int i = 0; i < 8; i++) qr[i] = ldp16(rq, (8 * t + i) * 1024 + d0 * 2);
    float res[8][8];
#pragma unroll
    for (int ch = 0; ch < 8; ch++) {
      v4u yy = *(const v4u*)(L + CV_Y + ch * CV_CH + (1 + (t >> 2)) * 80 + (t & 3) * 16);
      v4u kk = *(const v4u*)(L + CV_KV + ch * CV_CH + (1 + (t >> 2)) * 80 + (t & 3) * 16);
      float D = (ch & 1) ? bfhi(dv[ch >> 1]) : bflo(dv[ch >> 1]);
#pragma unroll
      for (int i = 0; i < 8; i++) {
        float y = (i & 1) ? bfhi(yy[i >> 1]) : bflo(yy[i >> 1]);
        float kvv = (i & 1) ? bfhi(kk[i >> 1]) : bflo(kk[i >> 1]);
        float qv = (ch & 1) ? bfhi(qr[i][ch >> 1]) : bflo(qr[i][ch >> 1]);
        res[i][ch] = qv * rbf(y + rbf(kvv * D));
      }
    }
#pragma unroll
    for (int i = 0; i < 8; i++) {
      v4u o = {pk(res[i][0], res[i][1]), pk(res[i][2], res[i][3]), pk(res[i][4], res[i][5]), pk(res[i][6], res[i][7])};
      stp16(ro, (8 * t + i) * 2048 + d0 * 2, o);
    }
    TRR(1); TRFLUSH(40);
  }
}

#define FUSED_SYM "_Z12fused_kernelPKvS0_S0_S0_S0_S0_S0_S0_S0_S0_S0_S0_PvPc"
#define INIT_SYM "_Z11init_kernelPcPKvS1_S1_S1_"
DI void touch_code(unsigned line0, unsigned stride, int n) {
  uint64_t a, e;
  asm volatile("s_getpc_b64 s[20:21]\n\ts_add_u32 s20, s20, " FUSED_SYM "@rel32@lo+4\n\ts_addc_u32 s21, s21, " FUSED_SYM "@rel32@hi+12\n\t"
               "s_mov_b64 %0, s[20:21]\n\t"
               "s_getpc_b64 s[20:21]\n\ts_add_u32 s20, s20, " INIT_SYM "@rel32@lo+4\n\ts_addc_u32 s21, s21, " INIT_SYM "@rel32@hi+12\n\t"
               "s_mov_b64 %1, s[20:21]" : "=s"(a), "=s"(e) :: "s20", "s21", "scc");
  unsigned acc = 0;
  for (int i = 0; i < n; i++) {
    const uint64_t p = a + (uint64_t)(line0 + i * stride) * 128;
    if (e > a && p + 4 <= e) acc ^= *(const volatile __attribute__((address_space(1))) unsigned*)p;
  }
  asm volatile("" :: "v"(acc));
}
__global__ void __launch_bounds__(256, 1)
fused_kernel(const void* Aq, const void* Ak, const void* Av, const void* Ashk, const void* Assk, const void* Assd,
             const void* Bx, const void* Bmem0, const void* Wq, const void* Wk, const void* Wv, const void* Wo, void* out,
             char* wsb) {
  __shared__ __attribute__((aligned(1024))) char lds[163840];
  const unsigned ldsb = (unsigned)(size_t)(lds_char_p)lds;
  rsrc_t ws = mkrsrc(wsb, WS_SPAN);
  const unsigned xph = *(const __attribute__((address_space(4))) unsigned*)(uintptr_t)&g_xph & 7;
  const int b = blockIdx.x, xcd = (b + xph) & 7, slot = b >> 3;
#define FSW flag_half(ws, xph)
#define TREXIT
  if (xcd < 4) {
    if (slot < 8) {
      chain_role(xcd * 8 + ((slot + H_ROT) & 7), OFF_PAR + ((8 - xph) & 7) * 16, ldsb, wsb, Bmem0, Wq, Wk, Wv, Wo);
      return;
    } else if (slot < 16) {
      touch_code((slot - 8) * 256 + threadIdx.x, 0, 1);
      const int h = xcd * 8 + slot - 8, lane = threadIdx.x & 63, w = __builtin_amdgcn_readfirstlane(threadIdx.x >> 6);
#pragma unroll
      for (int m = 0; m < 3; m++) {
        if (!((PF_WQ >> m) & 1)) continue;
        const v4u sq = mksrd(m == 0 ? Wq : m == 1 ? Wk : Wv, 512 * 4096 * 2);
#pragma unroll
        for (int i = 0; i < 32; i++)
          dma16(sq, lane * 16, ((i >> 1) * 32 + (slot - 8) * 4 + w) * 8192 + xcd * 2048 + (i & 1) * 1024, ldsb + w * 32768 + i * 1024);
      }
      for (int i = 0; i < PF_WO; i++) __builtin_amdgcn_s_sleep(32);
      const v4u srd = mksrd(Wo, 4096 * 512 * 2);
#pragma unroll
      for (int i = 0; i < 32; i++) dma16(srd, lane * 16, (h * 128 + w * 32 + i) * 1024, ldsb + w * 32768 + i * 1024);
      wait_vm<0>();
      return;
    }
  }
  {
    const int grp = xcd < 4 ? 0 : slot < 16 ? 1 : 2, x4 = xcd & 3, s16 = slot & 15;
    if (grp == 0) return;
    if (grp == 1) {
      cv_role(x4 * 16 + s16, lds, ws, Aq, Ak, Av, Ashk, Assk, Assd, out);
      __syncthreads();
      qa_role(s16 >> 3, x4 * 8 + (s16 & 7), lds, ldsb, ws, Bx, Wq, FSW);
    } else {
      const int h = x4 * 8 + (s16 & 7);
      if (s16 < 8) kv_role<0>(h, lds, ldsb, ws, Bx, Wk, FSW);
      else kv_role<1>(h, lds, ldsb, ws, Bx, Wv, FSW);
      __syncthreads();
      wo_role(x4 >> 1, x4 & 1, s16, lds, ldsb, ws, Wo, out, FSW);
    }
    TREXIT;
  }
}

#define stf16 st16
__global__ void __launch_bounds__(256) init_kernel(char* wsb, const void* Wq, const void* Wk, const void* Wv, const void* Wo) {
  const int t = threadIdx.x, b = blockIdx.x;
  const v4u ones = {POIS, POIS, POIS, POIS};
  rsrc_t ws = mkrsrc(wsb, WS_SPAN);
  if (b >= NPB) {
    if (b < NPB + 136) {
      const unsigned o = (b < NPB + 128 ? (b - NPB) * 8192 : OFF_MEM + (b - NPB - 128) * 8192) + t * 16;
      stf16(ws, o, ones);
      stf16(ws, o + 4096, ones);
    } else if (b < NPB + 168) {
      const unsigned o = ((b - NPB - 136) * NSEG + NSEG - NDIR) * 2048 + t * 16;
#pragma unroll
      for (int i = 0; i < NDIR / 2; i++) {
        stf16(ws, OFF_KM + o + i * 4096, ones);
        stf16(ws, OFF_VM + o + i * 4096, ones);
      }
    }
    return;
  }
  if (t >= 64) return;
  const int w = b >> 3;
  const unsigned addr = ((w >> 1) ? OFF_MEM + 131072 : OFF_MEM - 131072) + (w & 1) * 8192 + ((b & 7) * 4 + w) * 128;
  unsigned bst = ~0u;
#pragma unroll
  for (int k = 0; k < NPROBE; k++) {
    unsigned long long t0, t1;
    asm volatile("s_memtime %0\n\ts_waitcnt lgkmcnt(0)" : "=s"(t0) :: "memory");
    st4(ws, addr, POIS);
    asm volatile("s_waitcnt vmcnt(0)\n\ts_memtime %0\n\ts_waitcnt lgkmcnt(0)" : "=s"(t1) :: "memory");
    if (k) bst = min(bst, (unsigned)(t1 - t0));
  }
  if ((t & 63) == 0) st4(ws, OFF_PAR + (b & 7) * 16 + w * 4, bst);
  if (t == 0 && b == 0) {
    unsigned x;
    asm volatile("s_getreg_b32 %0, hwreg(HW_REG_XCC_ID, 0, 4)" : "=s"(x));
    __hip_atomic_store(&g_xph, x & 7, __ATOMIC_RELAXED, __HIP_MEMORY_SCOPE_AGENT);
  }
}

extern "C" void kernel_launch(void* const* d_in, const int* in_sizes, int n_in, void* d_out, int out_size, void* d_ws,
                              size_t ws_size, hipStream_t stream) {
  char* wsb = (char*)(((uintptr_t)d_ws + (2u << 20) - 1) & ~(uintptr_t)((2u << 20) - 1));
  init_kernel<<<NPB + NFILL, 256, 0, stream>>>(wsb, d_in[8], d_in[9], d_in[10], d_in[11]);
  fused_kernel<<<256, 256, 0, stream>>>(d_in[0], d_in[1], d_in[2], d_in[3], d_in[4], d_in[5], d_in[6], d_in[7], d_in[8],
                                        d_in[9], d_in[10], d_in[11], d_out, wsb);
}
```
